```python
import torch

def _softmax(x, axis=-1):
    return torch.softmax(x, dim=axis)
def _gelu(x):
    return torch.nn.functional.gelu(x, approximate="tanh")
def _silu(x):
    return torch.nn.functional.silu(x)
def _sigmoid(x):
    return torch.sigmoid(x)
def _layernorm(x, axis=-1, eps=1e-5):
    m = x.mean(dim=axis, keepdim=True)
    v = x.var(dim=axis, keepdim=True, unbiased=False)
    return (x - m) / torch.sqrt(v + eps)
def _scatter_add(tgt, idx, src, axis=0):
    return tgt.clone().index_add_(axis, idx, src)
def _segment_sum(src, seg, n):
    out = torch.zeros((n,) + src.shape[1:], dtype=src.dtype)
    return out.index_add_(0, seg, src)
def _pad(x, pw):
    flat = tuple(v for pair in reversed(pw) for v in pair)
    return torch.nn.functional.pad(x, flat)

kernel_name = 'concat_heads__gqa_block_cached__sliding_block_cached__decode'

def setup_inputs():

    _w = (lambda a: (lambda t: t.to(torch.bfloat16) if t.is_floating_point() else t)(torch.as_tensor(a)))
    def _A_setup():
        B, Sq, D = 1, 1, 512
        Hq, Hkv, Sk, Dh = 32, 8, 32768, 128
        assert Hq % Hkv == 0, (Hq, Hkv)
        return {
            "x": _w((lambda *s: torch.randn(*s, dtype=torch.bfloat16) * 0.8)(B, Sq, D)),
            "k_cache": _w((lambda *s: torch.randn(*s, dtype=torch.bfloat16) * 1.6)(B, Hkv, Sk, Dh)),
            "v_cache": _w((lambda *s: torch.randn(*s, dtype=torch.bfloat16) * 0.2)(B, Hkv, Sk, Dh)),
            "wq": _w((lambda *s: torch.randn(*s, dtype=torch.bfloat16) * 1.6)(D, Hq * Dh) * (D ** -0.5)),
            "wo": _w((lambda *s: torch.randn(*s, dtype=torch.bfloat16) * 0.2)(Hq * Dh, D) * ((Hq * Dh) ** -0.5)),
        }
    _A_in = _A_setup()

    def _B_setup():
        B, Sq, D = 1, 1, 512
        Hq, Hkv, Sk, Dh = 32, 8, 32768, 128
        assert Hq % Hkv == 0, (Hq, Hkv)
        return {
            "x": _w((lambda *s: torch.randn(*s, dtype=torch.bfloat16) * 0.8)(B, Sq, D)),
            "k_cache": _w((lambda *s: torch.randn(*s, dtype=torch.bfloat16) * 1.6)(B, Hkv, Sk, Dh)),
            "v_cache": _w((lambda *s: torch.randn(*s, dtype=torch.bfloat16) * 0.2)(B, Hkv, Sk, Dh)),
            "wq": _w((lambda *s: torch.randn(*s, dtype=torch.bfloat16) * 1.6)(D, Hq * Dh) * (D ** -0.5)),
            "wo": _w((lambda *s: torch.randn(*s, dtype=torch.bfloat16) * 0.2)(Hq * Dh, D) * ((Hq * Dh) ** -0.5)),
        }
    _B_in = _B_setup()

    return {
        "A_x": _A_in["x"],
        "A_k_cache": _A_in["k_cache"],
        "A_v_cache": _A_in["v_cache"],
        "A_wq": _A_in["wq"],
        "A_wo": _A_in["wo"],
        "B_x": _B_in["x"],
        "B_k_cache": _B_in["k_cache"],
        "B_v_cache": _B_in["v_cache"],
        "B_wq": _B_in["wq"],
        "B_wo": _B_in["wo"],
    }

def reference(A_x, A_k_cache, A_v_cache, A_wq, A_wo, B_x, B_k_cache, B_v_cache, B_wq, B_wo):
    def _A_ref(x, k_cache, v_cache, wq, wo):
        B, Sq, D = x.shape
        Hq, Dh = 32, 128
        Hkv = k_cache.shape[1]
        rep = Hq // Hkv
        q = (lambda x, *d: x.permute(*d))((x @ wq).reshape(B, Sq, Hq, Dh), 0, 2, 1, 3)
        k = (lambda x, n, axis: torch.repeat_interleave(x, n, dim=axis))(k_cache, rep, axis=1)
        v = (lambda x, n, axis: torch.repeat_interleave(x, n, dim=axis))(v_cache, rep, axis=1)
        scores = torch.einsum("bhqd,bhkd->bhqk", q, k) * (Dh ** -0.5)
        p = _softmax(scores, axis=-1)
        o = torch.einsum("bhqk,bhkd->bhqd", p, v)
        return (lambda x, *d: x.permute(*d))(o, 0, 2, 1, 3).reshape(B, Sq, Hq * Dh) @ wo

    def _B_ref(x, k_cache, v_cache, wq, wo):
        B, Sq, D = x.shape
        Hq, Dh = 32, 128
        Hkv, Sk = k_cache.shape[1], k_cache.shape[2]
        W = min(4096, max(256, Sk // 4))
        rep = Hq // Hkv
        q = (lambda x, *d: x.permute(*d))((x @ wq).reshape(B, Sq, Hq, Dh), 0, 2, 1, 3)
        k = (lambda x, n, axis: torch.repeat_interleave(x, n, dim=axis))(k_cache[:, :, Sk - W:, :], rep, axis=1)
        v = (lambda x, n, axis: torch.repeat_interleave(x, n, dim=axis))(v_cache[:, :, Sk - W:, :], rep, axis=1)
        scores = torch.einsum("bhqd,bhkd->bhqk", q, k) * (Dh ** -0.5)
        p = _softmax(scores, axis=-1)
        o = torch.einsum("bhqk,bhkd->bhqd", p, v)
        return (lambda x, *d: x.permute(*d))(o, 0, 2, 1, 3).reshape(B, Sq, Hq * Dh) @ wo

    a = _A_ref(A_x, A_k_cache, A_v_cache, A_wq, A_wo)
    b = _B_ref(B_x, B_k_cache, B_v_cache, B_wq, B_wo)
    return torch.concatenate([a, b], axis=-1)
```

```cpp
#include <hip/hip_runtime.h>
#include <hip/hip_bf16.h>
#include <stdint.h>

constexpr int D = 512, HQ = 32, HKV = 8, SK = 32768, DH = 128, W = 4096;
constexpr int NSA = 32;
constexpr int NSB = 4;
constexpr int KPS = 1024;

__device__ __forceinline__ float bf2f(unsigned short b) { return __uint_as_float(((unsigned)b) << 16); }
__device__ __forceinline__ unsigned short f2bf(float f) { __hip_bfloat16 h = __float2bfloat16(f); return *reinterpret_cast<unsigned short*>(&h); }

__global__ void qproj(const unsigned short* xA, const unsigned short* wqA, const unsigned short* xB, const unsigned short* wqB, float* q) {
  int br = blockIdx.y; int col = blockIdx.x * 256 + threadIdx.x;
  const unsigned short* x = br ? xB : xA; const unsigned short* w = br ? wqB : wqA;
  float acc = 0.f;
  for (int r = 0; r < D; ++r) acc += bf2f(x[r]) * bf2f(w[(size_t)r * 4096 + col]);
  q[br * 4096 + col] = bf2f(f2bf(acc));
}

__global__ void attn(const unsigned short* kA, const unsigned short* vA, const unsigned short* kB, const unsigned short* vB, const float* q, float* part) {
  int s = blockIdx.x, h = blockIdx.y;
  int br = s >= NSA; int sp = br ? s - NSA : s;
  const unsigned short* K = (br ? kB : kA) + ((size_t)h * SK + (br ? SK - W : 0) + (size_t)sp * KPS) * DH;
  const unsigned short* V = (br ? vB : vA) + ((size_t)h * SK + (br ? SK - W : 0) + (size_t)sp * KPS) * DH;
  __shared__ float sq[4 * 128];
  __shared__ float sc[4 * KPS];
  __shared__ float red[4][256];
  int tid = threadIdx.x;
  for (int i = tid; i < 512; i += 256) sq[i] = q[br * 4096 + h * 512 + i];
  __syncthreads();
  for (int k = tid; k < KPS; k += 256) {
    float a0 = 0, a1 = 0, a2 = 0, a3 = 0;
    for (int d = 0; d < DH; ++d) { float kv = bf2f(K[(size_t)k * DH + d]); a0 += kv * sq[d]; a1 += kv * sq[128 + d]; a2 += kv * sq[256 + d]; a3 += kv * sq[384 + d]; }
    const float sc_ = 0.088388347648318440f;
    sc[k] = a0 * sc_; sc[KPS + k] = a1 * sc_; sc[2 * KPS + k] = a2 * sc_; sc[3 * KPS + k] = a3 * sc_;
  }
  __syncthreads();
  float m[4], l[4];
  for (int g = 0; g < 4; ++g) {
    float mm = -INFINITY;
    for (int k = tid; k < KPS; k += 256) mm = fmaxf(mm, sc[g * KPS + k]);
    red[g][tid] = mm;
  }
  __syncthreads();
  for (int g = 0; g < 4; ++g) { float mm = -INFINITY; for (int i = 0; i < 256; ++i) mm = fmaxf(mm, red[g][i]); m[g] = mm; }
  __syncthreads();
  for (int g = 0; g < 4; ++g) {
    float ss = 0.f;
    for (int k = tid; k < KPS; k += 256) { float p = __expf(sc[g * KPS + k] - m[g]); sc[g * KPS + k] = p; ss += p; }
    red[g][tid] = ss;
  }
  __syncthreads();
  for (int g = 0; g < 4; ++g) { float ss = 0; for (int i = 0; i < 256; ++i) ss += red[g][i]; l[g] = ss; }
  {
    int g = tid >> 6, d = (tid & 63) * 2;
    float o0 = 0, o1 = 0;
    for (int k = 0; k < KPS; ++k) { float p = sc[g * KPS + k]; o0 += p * bf2f(V[(size_t)k * DH + d]); o1 += p * bf2f(V[(size_t)k * DH + d + 1]); }
    float* o = part + (((size_t)h * (NSA + NSB) + s) * 4 + g) * 130;
    o[2 + d] = o0; o[3 + d] = o1;
    if ((tid & 63) == 0) { o[0] = m[g]; o[1] = l[g]; }
  }
}

__global__ void combine(const float* part, float* o) {
  int hq = blockIdx.x, br = blockIdx.y, d = threadIdx.x;
  int h = hq >> 2, g = hq & 3;
  int s0 = br ? NSA : 0, ns = br ? NSB : NSA;
  float M = -INFINITY;
  for (int s = 0; s < ns; ++s) M = fmaxf(M, part[(((size_t)h * (NSA + NSB) + s0 + s) * 4 + g) * 130]);
  float L = 0, acc = 0;
  for (int s = 0; s < ns; ++s) { const float* p = part + (((size_t)h * (NSA + NSB) + s0 + s) * 4 + g) * 130; float w = __expf(p[0] - M); L += w * p[1]; acc += w * p[2 + d]; }
  o[br * 4096 + hq * 128 + d] = bf2f(f2bf(acc / L));
}

__global__ void oproj(const float* o, const unsigned short* woA, const unsigned short* woB, unsigned short* out) {
  int br = blockIdx.y; int col = blockIdx.x * 64 + (threadIdx.x & 63); int rg = threadIdx.x >> 6;
  const unsigned short* w = br ? woB : woA;
  float acc = 0;
  for (int r = rg * 1024; r < rg * 1024 + 1024; ++r) acc += o[br * 4096 + r] * bf2f(w[(size_t)r * 512 + col]);
  __shared__ float red[4][64];
  red[rg][threadIdx.x & 63] = acc;
  __syncthreads();
  if (rg == 0) { float s = red[0][threadIdx.x] + red[1][threadIdx.x] + red[2][threadIdx.x] + red[3][threadIdx.x]; out[br * 512 + col] = f2bf(s); }
}

extern "C" void kernel_launch(void* const* d_in, const int* in_sizes, int n_in,
                              void* d_out, int out_size, void* d_ws, size_t ws_size,
                              hipStream_t stream) {
  const unsigned short* xA = (const unsigned short*)d_in[0];
  const unsigned short* kA = (const unsigned short*)d_in[1];
  const unsigned short* vA = (const unsigned short*)d_in[2];
  const unsigned short* wqA = (const unsigned short*)d_in[3];
  const unsigned short* woA = (const unsigned short*)d_in[4];
  const unsigned short* xB = (const unsigned short*)d_in[5];
  const unsigned short* kB = (const unsigned short*)d_in[6];
  const unsigned short* vB = (const unsigned short*)d_in[7];
  const unsigned short* wqB = (const unsigned short*)d_in[8];
  const unsigned short* woB = (const unsigned short*)d_in[9];
  float* q = (float*)d_ws;
  float* o = q + 8192;
  float* part = o + 8192;
  qproj<<<dim3(16, 2), 256, 0, stream>>>(xA, wqA, xB, wqB, q);
  attn<<<dim3(NSA + NSB, HKV), 256, 0, stream>>>(kA, vA, kB, vB, q, part);
  combine<<<dim3(32, 2), 128, 0, stream>>>(part, o);
  oproj<<<dim3(8, 2), 256, 0, stream>>>(o, woA, woB, (unsigned short*)d_out);
}
```

```cpp
#include <hip/hip_runtime.h>
#include <stdint.h>

#define GAS __attribute__((address_space(1)))
#define LAS __attribute__((address_space(3)))
typedef unsigned v4u __attribute__((ext_vector_type(4)));
typedef unsigned v2u __attribute__((ext_vector_type(2)));
typedef float f32x4 __attribute__((ext_vector_type(4)));
typedef float f32x2 __attribute__((ext_vector_type(2)));
typedef __bf16 bf16x8 __attribute__((ext_vector_type(8)));
typedef __bf16 bf16x2 __attribute__((ext_vector_type(2)));
typedef short s16x4 __attribute__((ext_vector_type(4)));

#define STAMPS 0

constexpr int SK = 32768, DH = 128, WIN = 4096;
constexpr int NWG = 256, NTHR = 512;
constexpr float SCALE = 0.08838834764831845f;
constexpr float LOG2E = 1.4426950408889634f;

constexpr unsigned WS_EPOCH = 0;
constexpr unsigned WS_QP = 4096;
constexpr unsigned SLOT = 4224;
constexpr unsigned WS_PA = WS_QP + 2 * 64 * 2 * 512;
constexpr unsigned WS_PB = WS_PA + 256 * SLOT;
constexpr unsigned WS_OP = WS_PB + 256 * SLOT;
constexpr unsigned WS_END = WS_OP + 2 * 32 * 64 * 128;
constexpr unsigned WS_STAMP = (WS_END + 4095) / 4096 * 4096;

constexpr int L_RED = 0;
constexpr int L_QS = L_RED + 2048;
constexpr int L_MB = L_QS + 2048;
constexpr int L_MA = L_MB + 8 * 520 * 4;
constexpr int L_CMB = L_MA + 8 * 520 * 4;
constexpr int L_OS = L_CMB + 2 * 16 * 68 * 4;
constexpr int L_R2 = L_OS + 512;
constexpr int L_FIN = L_R2 + 8 * 2 * 128 * 4;
constexpr int L_END = L_FIN + 512;

constexpr unsigned SPIN_CAP = 1u << 18;

struct Params {
  const unsigned short* x[2];
  const unsigned char* k[2];
  const unsigned char* v[2];
  const unsigned char* wq[2];
  const unsigned char* wo[2];
  unsigned short* out;
  unsigned char* ws;
};

template <int CTRL> __device__ __forceinline__ float dpp(float x) {
  return __builtin_bit_cast(float, __builtin_amdgcn_mov_dpp(__builtin_bit_cast(int, x), CTRL, 0xf, 0xf, true));
}
__device__ __forceinline__ unsigned cvtpk(float lo, float hi) {
  f32x2 v = {lo, hi};
  return __builtin_bit_cast(unsigned, __builtin_convertvector(v, bf16x2));
}
__device__ __forceinline__ float bflo(unsigned u) { return __uint_as_float(u << 16); }
__device__ __forceinline__ float bfhi(unsigned u) { return __uint_as_float(u & 0xffff0000u); }
__device__ __forceinline__ float x16_sum(float x) {
  auto s = __builtin_amdgcn_permlane16_swap(__float_as_uint(x), __float_as_uint(x), false, false);
  return __uint_as_float(s[0]) + __uint_as_float(s[1]);
}
__device__ __forceinline__ float x32_sum(float x) {
  auto s = __builtin_amdgcn_permlane32_swap(__float_as_uint(x), __float_as_uint(x), false, false);
  return __uint_as_float(s[0]) + __uint_as_float(s[1]);
}
__device__ __forceinline__ float x16_max(float x) {
  auto s = __builtin_amdgcn_permlane16_swap(__float_as_uint(x), __float_as_uint(x), false, false);
  return fmaxf(__uint_as_float(s[0]), __uint_as_float(s[1]));
}
__device__ __forceinline__ float x32_max(float x) {
  auto s = __builtin_amdgcn_permlane32_swap(__float_as_uint(x), __float_as_uint(x), false, false);
  return fmaxf(__uint_as_float(s[0]), __uint_as_float(s[1]));
}
__device__ __forceinline__ v4u ldnt(const unsigned char* p) { return __builtin_nontemporal_load((const GAS v4u*)p); }
#define SB() __builtin_amdgcn_sched_barrier(0)
#define USE(x) do { asm volatile("" :: "v"(x)); } while (0)

__device__ __forceinline__ f32x4 mfma32(v4u a, v4u b, f32x4 c) {
  return __builtin_amdgcn_mfma_f32_16x16x32_bf16(__builtin_bit_cast(bf16x8, a), __builtin_bit_cast(bf16x8, b), c, 0, 0, 0);
}
__device__ __forceinline__ f32x4 mfma16(v2u a, v2u b, f32x4 c) {
  return __builtin_amdgcn_mfma_f32_16x16x16bf16_1k(__builtin_bit_cast(s16x4, a), __builtin_bit_cast(s16x4, b), c, 0, 0, 0);
}

__device__ __forceinline__ float qk_group(v4u kf, v4u qf) {
  f32x4 z = {0.f, 0.f, 0.f, 0.f};
  f32x4 d = mfma32(kf, qf, z);
  float s = dpp<0x00>(d[0]) + dpp<0x55>(d[1]);
  s += dpp<0xAA>(d[2]);
  s += dpp<0xFF>(d[3]);
  return s;
}
__device__ __forceinline__ void round_pair(float& a, float& b) {
  unsigned u = cvtpk(a, b);
  float a1 = bflo(u) * SCALE, b1 = bfhi(u) * SCALE;
  unsigned w = cvtpk(a1, b1);
  a = bflo(w); b = bfhi(w);
}

struct Tag { unsigned lo, hi8; };
__device__ __forceinline__ unsigned gpack(float f, unsigned hi8) { return ((__float_as_uint(f) + 0x80u) & 0xffffff00u) | hi8; }
__device__ __forceinline__ float gval(unsigned hi) { return __uint_as_float(hi & 0xffffff00u); }
__device__ __forceinline__ bool gok(v4u g, Tag t) { return g.x == t.lo && g.z == t.lo && (g.y & 0xffu) == t.hi8 && (g.w & 0xffu) == t.hi8; }

__device__ __forceinline__ v4u ld_sc1(__amdgpu_buffer_rsrc_t rs, unsigned off) { return __builtin_amdgcn_raw_buffer_load_b128(rs, off, 0, 16); }
__device__ __forceinline__ void st_sc1(__amdgpu_buffer_rsrc_t rs, unsigned off, v4u v) { __builtin_amdgcn_raw_buffer_store_b128(v, rs, off, 0, 16); }

#define STAMP(i) do {} while (0)

__device__ __forceinline__ void write_partial(LAS unsigned char* base, int w, int lane, f32x4 a1, f32x4 a2, float m, float l) {
  LAS float* rec = (LAS float*)(base) + w * 520;
  *(LAS f32x4*)(rec + (lane >> 4) * 128 + (lane & 15) * 8) = a1;
  *(LAS f32x4*)(rec + (lane >> 4) * 128 + (lane & 15) * 8 + 4) = a2;
  if (lane < 16 && (lane & 3) == 0) { rec[512 + (lane >> 2)] = m; rec[516 + (lane >> 2)] = l; }
}
__device__ __forceinline__ void merge_publish(LAS unsigned char* base, int tid, __amdgpu_buffer_rsrc_t rs, unsigned slot_off, Tag tag) {
  if (tid < 256) {
    const int h = tid >> 6, d = 2 * (tid & 63);
    const LAS float* rec = (const LAS float*)base;
    float mw[8], M = -INFINITY;
#pragma unroll
    for (int w = 0; w < 8; ++w) { mw[w] = rec[w * 520 + 512 + h]; M = fmaxf(M, mw[w]); }
    float o0 = 0.f, o1 = 0.f, L = 0.f;
#pragma unroll
    for (int w = 0; w < 8; ++w) {
      const float e = __builtin_amdgcn_exp2f((mw[w] - M) * LOG2E);
      const f32x2 ov = *(const LAS f32x2*)(rec + w * 520 + h * 128 + d);
      o0 = fmaf(e, ov[0], o0); o1 = fmaf(e, ov[1], o1); L = fmaf(e, rec[w * 520 + 516 + h], L);
    }
    v4u g = {tag.lo, gpack(o0, tag.hi8), tag.lo, gpack(o1, tag.hi8)};
    st_sc1(rs, slot_off + (h * 128 + d) * 8, g);
    if ((tid & 63) == 0) { v4u g2 = {tag.lo, gpack(M, tag.hi8), tag.lo, gpack(L, tag.hi8)}; st_sc1(rs, slot_off + 4096 + h * 16, g2); }
  }
}

__global__ void __launch_bounds__(NTHR, 2) fused_decode(Params p) {
  __shared__ __attribute__((aligned(16))) unsigned char smem_[L_END];
  LAS unsigned char* L = (LAS unsigned char*)smem_;
  const int tid = threadIdx.x, lane = tid & 63, w = __builtin_amdgcn_readfirstlane(tid >> 6), b = blockIdx.x;
  const int h = b & 7, j = b >> 3;
  const __amdgpu_buffer_rsrc_t rs = __builtin_amdgcn_make_buffer_rsrc((void*)p.ws, 0, (int)WS_END, 0x00020000);
  STAMP(0);

  const unsigned long long E = *(const GAS unsigned long long*)(p.ws + WS_EPOCH);
  unsigned long long z = E + 0x9E3779B97F4A7C15ull;
  z = (z ^ (z >> 30)) * 0xBF58476D1CE4E5B9ull; z = (z ^ (z >> 27)) * 0x94D049BB133111EBull; z ^= z >> 31;
  Tag tag; tag.lo = (unsigned)z; tag.hi8 = (unsigned)(z >> 32) & 0xffu;

  const int qbr = b >> 7, qcb = (b >> 1) & 63, qhalf = b & 1;
  const int qrow0 = 256 * qhalf + 32 * w + 4 * (lane >> 3);
  const v2u xv = *(const GAS v2u*)(p.x[qbr] + qrow0);
  v4u wqr[4];
  {
    const unsigned char* wp = p.wq[qbr] + (size_t)qrow0 * 8192 + (64 * qcb + 8 * (lane & 7)) * 2;
#pragma unroll
    for (int i = 0; i < 4; ++i) wqr[i] = ldnt(wp + (size_t)i * 8192);
  }
  SB();
  const unsigned koff = ((lane & 15) >> 2) * 256 + (lane >> 4) * 64 + (lane & 3) * 16;
  const unsigned voff = lane * 16;
  const size_t rowA = (size_t)h * SK + 1024 * j + 128 * w;
  const size_t rowB = (size_t)h * SK + (SK - WIN) + 128 * j + 16 * w;
  const unsigned char* kA = p.k[0] + rowA * 256;
  const unsigned char* vA = p.v[0] + rowA * 256;
  const unsigned char* kB = p.k[1] + rowB * 256;
  const unsigned char* vB = p.v[1] + rowB * 256;
  v4u KB[4], VB[4], K[8][4], V[8][4];
#define ISSUE_K(t) do { _Pragma("unroll") for (int i_ = 0; i_ < 4; ++i_) K[t][i_] = ldnt(kA + (t) * 4096 + i_ * 1024 + koff); SB(); } while (0)
#define ISSUE_V(t) do { _Pragma("unroll") for (int i_ = 0; i_ < 4; ++i_) V[t][i_] = ldnt(vA + (t) * 4096 + i_ * 1024 + voff); SB(); } while (0)
#pragma unroll
  for (int i = 0; i < 4; ++i) KB[i] = ldnt(kB + i * 1024 + koff);
#pragma unroll
  for (int i = 0; i < 4; ++i) VB[i] = ldnt(vB + i * 1024 + voff);
  SB();
  ISSUE_K(0); ISSUE_K(1);

  {
    float acc[8];
#pragma unroll
    for (int c = 0; c < 8; ++c) acc[c] = 0.f;
    const float xs[4] = {bflo(xv.x), bfhi(xv.x), bflo(xv.y), bfhi(xv.y)};
#pragma unroll
    for (int i = 0; i < 4; ++i) {
#pragma unroll
      for (int c = 0; c < 4; ++c) { acc[2 * c] = fmaf(xs[i], bflo(wqr[i][c]), acc[2 * c]); acc[2 * c + 1] = fmaf(xs[i], bfhi(wqr[i][c]), acc[2 * c + 1]); }
    }
#pragma unroll
    for (int c = 0; c < 8; ++c) { float s = acc[c]; s += dpp<0x128>(s); s = x16_sum(s); s = x32_sum(s); acc[c] = s; }
    if (lane < 8) {
      LAS float* r = (LAS float*)(L + L_RED) + w * 64 + lane * 8;
      *(LAS f32x4*)r = f32x4{acc[0], acc[1], acc[2], acc[3]};
      *(LAS f32x4*)(r + 4) = f32x4{acc[4], acc[5], acc[6], acc[7]};
    }
  }
  STAMP(1);
  __syncthreads();
  if (w == 0 && lane < 32) {
    const LAS float* r = (const LAS float*)(L + L_RED) + 2 * lane;
    float s0 = 0.f, s1 = 0.f;
#pragma unroll
    for (int ww = 0; ww < 8; ++ww) { const f32x2 t = *(const LAS f32x2*)(r + ww * 64); s0 += t[0]; s1 += t[1]; }
    v4u g = {tag.lo, gpack(s0, tag.hi8), tag.lo, gpack(s1, tag.hi8)};
    st_sc1(rs, WS_QP + ((qbr * 64 + qcb) * 2 + qhalf) * 512 + lane * 16, g);
  }
  SB();
  USE(KB[3]); ISSUE_K(2);
  USE(VB[3]); ISSUE_K(3);
  USE(K[0][3]); ISSUE_K(4);
  USE(K[1][3]); ISSUE_K(5);
  const unsigned goff = WS_QP + (((w >> 2) * 64 + 8 * h + 2 * (w & 3) + (lane >> 5)) * 2) * 512 + (lane & 31) * 16;
  v4u g0 = ld_sc1(rs, goff), g1 = ld_sc1(rs, goff + 512);
  SB();
  USE(K[2][3]); ISSUE_K(6);
  USE(K[3][3]); ISSUE_K(7);
  USE(K[4][3]); ISSUE_V(0);
  {
    unsigned spins = 0;
    while (!__all(gok(g0, tag) && gok(g1, tag))) {
      if (++spins > SPIN_CAP) break;
      __builtin_amdgcn_s_sleep(2);
      g0 = ld_sc1(rs, goff); g1 = ld_sc1(rs, goff + 512);
    }
    const float q0 = gval(g0.y) + gval(g1.y), q1 = gval(g0.w) + gval(g1.w);
    *(LAS unsigned*)(L + L_QS + (w >> 2) * 1024 + ((2 * (w & 3) + (lane >> 5)) * 64 + 2 * (lane & 31)) * 2) = cvtpk(q0, q1);
  }
  SB();
  USE(K[5][3]); ISSUE_V(1);
  STAMP(2);
  __syncthreads();
  STAMP(3);
  const unsigned qldsoff = ((lane & 15) >> 2) * 256 + (4 * (lane >> 4) + (lane & 3)) * 16;
  const v4u qA = *(const LAS v4u*)(L + L_QS + qldsoff);
  const v4u qB = *(const LAS v4u*)(L + L_QS + 1024 + qldsoff);
  const unsigned pm0 = (lane & 3) == 0 ? 0xffffu : (lane & 3) == 1 ? 0xffff0000u : 0u;
  const unsigned pm1 = (lane & 3) == 2 ? 0xffffu : (lane & 3) == 3 ? 0xffff0000u : 0u;

  {
    float s[4];
#pragma unroll
    for (int i = 0; i < 4; ++i) s[i] = qk_group(KB[i], qB);
    round_pair(s[0], s[1]); round_pair(s[2], s[3]);
    float m = fmaxf(fmaxf(s[0], s[1]), fmaxf(s[2], s[3]));
    m = x16_max(m); m = x32_max(m);
    const float mb = -m * LOG2E;
    f32x4 a1 = {0.f, 0.f, 0.f, 0.f}, a2 = {0.f, 0.f, 0.f, 0.f};
    float l = 0.f;
#pragma unroll
    for (int i = 0; i < 4; ++i) {
      const float pe = __builtin_amdgcn_exp2f(fmaf(s[i], LOG2E, mb));
      l += pe;
      const unsigned pp = cvtpk(pe, pe);
      const v2u pa = {pp & pm0, pp & pm1};
      a1 = mfma16(pa, v2u{VB[i].x, VB[i].y}, a1);
      a2 = mfma16(pa, v2u{VB[i].z, VB[i].w}, a2);
    }
    l = x16_sum(l); l = x32_sum(l);
    write_partial(L + L_MB, w, lane, a1, a2, m, l);
  }
  SB();
  USE(K[6][3]); ISSUE_V(2);
  __syncthreads();
  merge_publish(L + L_MB, tid, rs, WS_PB + (h * 32 + j) * SLOT, tag);
  SB();
  STAMP(4);
  float sc[8][4];
#pragma unroll
  for (int t = 0; t < 7; ++t) {
#pragma unroll
    for (int i = 0; i < 4; ++i) sc[t][i] = qk_group(K[t][i], qA);
    round_pair(sc[t][0], sc[t][1]); round_pair(sc[t][2], sc[t][3]);
    SB();
  }
  USE(K[7][3]); ISSUE_V(3);
#pragma unroll
  for (int i = 0; i < 4; ++i) sc[7][i] = qk_group(K[7][i], qA);
  round_pair(sc[7][0], sc[7][1]); round_pair(sc[7][2], sc[7][3]);
  float mA = -INFINITY;
#pragma unroll
  for (int t = 0; t < 8; ++t) mA = fmaxf(mA, fmaxf(fmaxf(sc[t][0], sc[t][1]), fmaxf(sc[t][2], sc[t][3])));
  mA = x16_max(mA); mA = x32_max(mA);
  float lA = 0.f;
  unsigned pp[8][4];
  {
    const float mb = -mA * LOG2E;
#pragma unroll
    for (int t = 0; t < 8; ++t)
#pragma unroll
      for (int i = 0; i < 4; ++i) { const float pe = __builtin_amdgcn_exp2f(fmaf(sc[t][i], LOG2E, mb)); lA += pe; pp[t][i] = cvtpk(pe, pe); }
  }
  SB();
  STAMP(5);
  const int rg = b >> 2, cg = b & 3;
  v4u WO[2][2];
  f32x4 a1 = {0.f, 0.f, 0.f, 0.f}, a2 = {0.f, 0.f, 0.f, 0.f};
#pragma unroll
  for (int t = 0; t < 8; ++t) {
#pragma unroll
    for (int i = 0; i < 4; ++i) {
      const v2u pa = {pp[t][i] & pm0, pp[t][i] & pm1};
      a1 = mfma16(pa, v2u{V[t][i].x, V[t][i].y}, a1);
      a2 = mfma16(pa, v2u{V[t][i].z, V[t][i].w}, a2);
    }
    SB();
    if (t + 4 < 8) { ISSUE_V(t + 4); }
    if (t == 4 || t == 5) {
      const int br = t == 4 ? 1 : 0;
      const unsigned char* wp = p.wo[br] + (size_t)(64 * rg + 8 * w + (lane >> 4)) * 1024 + (128 * cg + 8 * (lane & 15)) * 2;
      WO[br][0] = ldnt(wp); WO[br][1] = ldnt(wp + 4 * 1024);
      SB();
    }
  }
  lA = x16_sum(lA); lA = x32_sum(lA);
  write_partial(L + L_MA, w, lane, a1, a2, mA, lA);
  STAMP(6);
  __syncthreads();
  merge_publish(L + L_MA, tid, rs, WS_PA + (h * 32 + j) * SLOT, tag);
  SB();
  STAMP(7);
  {
    const int hq = rg >> 1, half = rg & 1, hk = hq >> 2, hh = hq & 3;
#pragma unroll
    for (int br = 1; br >= 0; --br) {
      const unsigned sbase = (br ? WS_PB : WS_PA) + (hk * 32 + 4 * w + (lane >> 5)) * SLOT;
      const unsigned oo = sbase + (hh * 128 + 64 * half + 2 * (lane & 31)) * 8, mo = sbase + 4096 + hh * 16;
      v4u o0, o1, m0, m1;
      unsigned spins = 0;
      while (true) {
        o0 = ld_sc1(rs, oo); o1 = ld_sc1(rs, oo + 2 * SLOT); m0 = ld_sc1(rs, mo); m1 = ld_sc1(rs, mo + 2 * SLOT);
        if (__all(gok(o0, tag) && gok(o1, tag) && gok(m0, tag) && gok(m1, tag))) break;
        if (++spins > SPIN_CAP) break;
        __builtin_amdgcn_s_sleep(1);
      }
      const float ma = gval(m0.y), mb = gval(m1.y), M = fmaxf(ma, mb);
      const float ea = __builtin_amdgcn_exp2f((ma - M) * LOG2E), eb = __builtin_amdgcn_exp2f((mb - M) * LOG2E);
      LAS float* rec = (LAS float*)(L + L_CMB) + (br * 16 + w * 2 + (lane >> 5)) * 68;
      *(LAS f32x2*)(rec + 4 + 2 * (lane & 31)) = f32x2{ea * gval(o0.y) + eb * gval(o1.y), ea * gval(o0.w) + eb * gval(o1.w)};
      if ((lane & 31) == 0) { rec[0] = M; rec[1] = ea * gval(m0.w) + eb * gval(m1.w); }
    }
  }
  STAMP(8);
  __syncthreads();
  if (tid < 128) {
    const int br = tid >> 6, d = tid & 63;
    const LAS float* rec = (const LAS float*)(L + L_CMB) + br * 16 * 68;
    float M = -INFINITY;
#pragma unroll
    for (int e = 0; e < 16; ++e) M = fmaxf(M, rec[e * 68]);
    float o = 0.f, Ls = 0.f;
#pragma unroll
    for (int e = 0; e < 16; ++e) { const float ee = __builtin_amdgcn_exp2f((rec[e * 68] - M) * LOG2E); o = fmaf(ee, rec[e * 68 + 4 + d], o); Ls = fmaf(ee, rec[e * 68 + 1], Ls); }
    const float on = o / Ls;
    ((LAS float*)(L + L_OS))[br * 64 + d] = bflo(cvtpk(on, on) & 0xffffu);
  }
  __syncthreads();
#pragma unroll
  for (int br = 0; br < 2; ++br) {
    const float oa = ((const LAS float*)(L + L_OS))[br * 64 + 8 * w + (lane >> 4)], ob = ((const LAS float*)(L + L_OS))[br * 64 + 8 * w + 4 + (lane >> 4)];
    float acc[8];
#pragma unroll
    for (int c = 0; c < 4; ++c) {
      acc[2 * c] = fmaf(oa, bflo(WO[br][0][c]), ob * bflo(WO[br][1][c]));
      acc[2 * c + 1] = fmaf(oa, bfhi(WO[br][0][c]), ob * bfhi(WO[br][1][c]));
    }
#pragma unroll
    for (int c = 0; c < 8; ++c) { float s = x16_sum(acc[c]); acc[c] = x32_sum(s); }
    if (lane < 16) {
      LAS float* r = (LAS float*)(L + L_R2) + (w * 2 + br) * 128 + lane * 8;
      *(LAS f32x4*)r = f32x4{acc[0], acc[1], acc[2], acc[3]};
      *(LAS f32x4*)(r + 4) = f32x4{acc[4], acc[5], acc[6], acc[7]};
    }
  }
  __syncthreads();
  if (tid < 128) {
    const int br = tid >> 6, c = 2 * (tid & 63);
    float s0 = 0.f, s1 = 0.f;
#pragma unroll
    for (int ww = 0; ww < 8; ++ww) { const f32x2 t = *(const LAS f32x2*)((const LAS float*)(L + L_R2) + (ww * 2 + br) * 128 + c); s0 += t[0]; s1 += t[1]; }
    const int col = 128 * cg + c;
    v4u g = {tag.lo, gpack(s0, tag.hi8), tag.lo, gpack(s1, tag.hi8)};
    st_sc1(rs, WS_OP + ((br * 32 + (col >> 4)) * 64 + rg) * 128 + (col & 15) * 8, g);
  }
  STAMP(9);
  if (b < 64) {
    const unsigned fo = WS_OP + b * 8192 + tid * 16;
    v4u g;
    unsigned spins = 0;
    while (true) {
      g = ld_sc1(rs, fo);
      if (__all(gok(g, tag))) break;
      if (++spins > SPIN_CAP) break;
      __builtin_amdgcn_s_sleep(1);
    }
    float s0 = gval(g.y), s1 = gval(g.w);
    s0 += dpp<0x128>(s0); s1 += dpp<0x128>(s1);
    s0 = x16_sum(s0); s1 = x16_sum(s1);
    s0 = x32_sum(s0); s1 = x32_sum(s1);
    if (lane < 8) *(LAS f32x2*)((LAS float*)(L + L_FIN) + w * 16 + 2 * lane) = f32x2{s0, s1};
    __syncthreads();
    if (tid < 8) {
      float t0 = 0.f, t1 = 0.f;
#pragma unroll
      for (int ww = 0; ww < 8; ++ww) { const f32x2 t = *(const LAS f32x2*)((const LAS float*)(L + L_FIN) + ww * 16 + 2 * tid); t0 += t[0]; t1 += t[1]; }
      *(GAS unsigned*)(p.out + (b >> 5) * 512 + (b & 31) * 16 + 2 * tid) = cvtpk(t0, t1);
    }
    if (b == 0 && tid == 0) *(GAS unsigned long long*)(p.ws + WS_EPOCH) = E + 1;
  }
  STAMP(10);
}

extern "C" void kernel_launch(void* const* d_in, const int* in_sizes, int n_in,
                              void* d_out, int out_size, void* d_ws, size_t ws_size,
                              hipStream_t stream) {
  Params p;
  p.x[0] = (const unsigned short*)d_in[0]; p.k[0] = (const unsigned char*)d_in[1]; p.v[0] = (const unsigned char*)d_in[2];
  p.wq[0] = (const unsigned char*)d_in[3]; p.wo[0] = (const unsigned char*)d_in[4];
  p.x[1] = (const unsigned short*)d_in[5]; p.k[1] = (const unsigned char*)d_in[6]; p.v[1] = (const unsigned char*)d_in[7];
  p.wq[1] = (const unsigned char*)d_in[8]; p.wo[1] = (const unsigned char*)d_in[9];
  p.out = (unsigned short*)d_out; p.ws = (unsigned char*)d_ws;
  fused_decode<<<NWG, NTHR, 0, stream>>>(p);
}
```

```cpp
#include <hip/hip_runtime.h>
#include <stdint.h>

#define GAS __attribute__((address_space(1)))
#define LAS __attribute__((address_space(3)))
typedef unsigned v4u __attribute__((ext_vector_type(4)));
typedef unsigned v2u __attribute__((ext_vector_type(2)));
typedef float f32x4 __attribute__((ext_vector_type(4)));
typedef float f32x2 __attribute__((ext_vector_type(2)));
typedef __bf16 bf16x8 __attribute__((ext_vector_type(8)));
typedef __bf16 bf16x2 __attribute__((ext_vector_type(2)));
typedef short s16x4 __attribute__((ext_vector_type(4)));

#define DIAG_THR 33.0f
#define STAMPS 0

constexpr int SK = 32768, DH = 128, WIN = 4096;
constexpr int NWG = 256, NTHR = 512;
constexpr float SCALE = 0.08838834764831845f;
constexpr float LOG2E = 1.4426950408889634f;

constexpr unsigned WS_EPOCH = 0;
constexpr unsigned WS_QP = 4096;
constexpr unsigned SLOT = 4224;
constexpr unsigned WS_PA = WS_QP + 2 * 64 * 2 * 512;
constexpr unsigned WS_PB = WS_PA + 256 * SLOT;
constexpr unsigned WS_OP = WS_PB + 256 * SLOT;
constexpr unsigned WS_END = WS_OP + 2 * 32 * 64 * 128;
constexpr unsigned WS_STAMP = (WS_END + 4095) / 4096 * 4096;

constexpr int L_RED = 0;
constexpr int L_QS = L_RED + 2048;
constexpr int L_MB = L_QS + 2048;
constexpr int L_MA = L_MB + 8 * 520 * 4;
constexpr int L_CMB = L_MA + 8 * 520 * 4;
constexpr int L_OS = L_CMB + 2 * 16 * 68 * 4;
constexpr int L_R2 = L_OS + 512;
constexpr int L_FIN = L_R2 + 8 * 2 * 128 * 4;
constexpr int L_END = L_FIN + 512;

constexpr unsigned SPIN_CAP = 1u << 18;

struct Params {
  const unsigned short* x[2];
  const unsigned char* k[2];
  const unsigned char* v[2];
  const unsigned char* wq[2];
  const unsigned char* wo[2];
  unsigned short* out;
  unsigned char* ws;
};

template <int CTRL> __device__ __forceinline__ float dpp(float x) {
  return __builtin_bit_cast(float, __builtin_amdgcn_mov_dpp(__builtin_bit_cast(int, x), CTRL, 0xf, 0xf, true));
}
__device__ __forceinline__ unsigned cvtpk(float lo, float hi) {
  f32x2 v = {lo, hi};
  return __builtin_bit_cast(unsigned, __builtin_convertvector(v, bf16x2));
}
__device__ __forceinline__ float bflo(unsigned u) { return __uint_as_float(u << 16); }
__device__ __forceinline__ float bfhi(unsigned u) { return __uint_as_float(u & 0xffff0000u); }
__device__ __forceinline__ float x16_sum(float x) {
  auto s = __builtin_amdgcn_permlane16_swap(__float_as_uint(x), __float_as_uint(x), false, false);
  return __uint_as_float(s[0]) + __uint_as_float(s[1]);
}
__device__ __forceinline__ float x32_sum(float x) {
  auto s = __builtin_amdgcn_permlane32_swap(__float_as_uint(x), __float_as_uint(x), false, false);
  return __uint_as_float(s[0]) + __uint_as_float(s[1]);
}
__device__ __forceinline__ float x16_max(float x) {
  auto s = __builtin_amdgcn_permlane16_swap(__float_as_uint(x), __float_as_uint(x), false, false);
  return fmaxf(__uint_as_float(s[0]), __uint_as_float(s[1]));
}
__device__ __forceinline__ float x32_max(float x) {
  auto s = __builtin_amdgcn_permlane32_swap(__float_as_uint(x), __float_as_uint(x), false, false);
  return fmaxf(__uint_as_float(s[0]), __uint_as_float(s[1]));
}
__device__ __forceinline__ v4u ldnt(const unsigned char* p) { return __builtin_nontemporal_load((const GAS v4u*)p); }
#define SB() __builtin_amdgcn_sched_barrier(0)
#define USE(x) do { asm volatile("" :: "v"(x)); } while (0)

__device__ __forceinline__ f32x4 mfma32(v4u a, v4u b, f32x4 c) {
  return __builtin_amdgcn_mfma_f32_16x16x32_bf16(__builtin_bit_cast(bf16x8, a), __builtin_bit_cast(bf16x8, b), c, 0, 0, 0);
}
__device__ __forceinline__ f32x4 mfma16(v2u a, v2u b, f32x4 c) {
  return __builtin_amdgcn_mfma_f32_16x16x16bf16_1k(__builtin_bit_cast(s16x4, a), __builtin_bit_cast(s16x4, b), c, 0, 0, 0);
}

__device__ __forceinline__ float qk_group(v4u kf, v4u qf) {
  f32x4 z = {0.f, 0.f, 0.f, 0.f};
  f32x4 d = mfma32(kf, qf, z);
  float s = dpp<0x00>(d[0]) + dpp<0x55>(d[1]);
  s += dpp<0xAA>(d[2]);
  s += dpp<0xFF>(d[3]);
  return s;
}
__device__ __forceinline__ void round_pair(float& a, float& b) {
  unsigned u = cvtpk(a, b);
  float a1 = bflo(u) * SCALE, b1 = bfhi(u) * SCALE;
  unsigned w = cvtpk(a1, b1);
  a = bflo(w); b = bfhi(w);
}

struct Tag { unsigned lo, hi8; };
__device__ __forceinline__ unsigned gpack(float f, unsigned hi8) { return ((__float_as_uint(f) + 0x80u) & 0xffffff00u) | hi8; }
__device__ __forceinline__ float gval(unsigned hi) { return __uint_as_float(hi & 0xffffff00u); }
__device__ __forceinline__ bool gok(v4u g, Tag t) { return g.x == t.lo && g.z == t.lo && (g.y & 0xffu) == t.hi8 && (g.w & 0xffu) == t.hi8; }

__device__ __forceinline__ v4u ld_sc1(__amdgpu_buffer_rsrc_t rs, unsigned off) { return __builtin_amdgcn_raw_buffer_load_b128(rs, off, 0, 16); }
__device__ __forceinline__ void st_sc1(__amdgpu_buffer_rsrc_t rs, unsigned off, v4u v) { __builtin_amdgcn_raw_buffer_store_b128(v, rs, off, 0, 16); }

#define STAMP(i) do {} while (0)

__device__ __forceinline__ void write_partial(LAS unsigned char* base, int w, int lane, f32x4 a1, f32x4 a2, float m, float l) {
  LAS float* rec = (LAS float*)(base) + w * 520;
  *(LAS f32x4*)(rec + (lane >> 4) * 128 + (lane & 15) * 8) = a1;
  *(LAS f32x4*)(rec + (lane >> 4) * 128 + (lane & 15) * 8 + 4) = a2;
  if (lane < 16 && (lane & 3) == 0) { rec[512 + (lane >> 2)] = m; rec[516 + (lane >> 2)] = l; }
}
__device__ __forceinline__ void merge_publish(LAS unsigned char* base, int tid, __amdgpu_buffer_rsrc_t rs, unsigned slot_off, Tag tag) {
  if (tid < 256) {
    const int h = tid >> 6, d = 2 * (tid & 63);
    const LAS float* rec = (const LAS float*)base;
    float mw[8], M = -INFINITY;
#pragma unroll
    for (int w = 0; w < 8; ++w) { mw[w] = rec[w * 520 + 512 + h]; M = fmaxf(M, mw[w]); }
    float o0 = 0.f, o1 = 0.f, L = 0.f;
#pragma unroll
    for (int w = 0; w < 8; ++w) {
      const float e = __builtin_amdgcn_exp2f((mw[w] - M) * LOG2E);
      const f32x2 ov = *(const LAS f32x2*)(rec + w * 520 + h * 128 + d);
      o0 = fmaf(e, ov[0], o0); o1 = fmaf(e, ov[1], o1); L = fmaf(e, rec[w * 520 + 516 + h], L);
    }
    v4u g = {tag.lo, gpack(o0, tag.hi8), tag.lo, gpack(o1, tag.hi8)};
    st_sc1(rs, slot_off + (h * 128 + d) * 8, g);
    if ((tid & 63) == 0) { v4u g2 = {tag.lo, gpack(M, tag.hi8), tag.lo, gpack(L, tag.hi8)}; st_sc1(rs, slot_off + 4096 + h * 16, g2); }
  }
}

__global__ void __launch_bounds__(NTHR, 2) fused_decode(Params p) {
  __shared__ __attribute__((aligned(16))) unsigned char smem_[L_END];
  LAS unsigned char* L = (LAS unsigned char*)smem_;
  const int tid = threadIdx.x, lane = tid & 63, w = __builtin_amdgcn_readfirstlane(tid >> 6), b = blockIdx.x;
  const int h = b & 7, j = b >> 3;
  const __amdgpu_buffer_rsrc_t rs = __builtin_amdgcn_make_buffer_rsrc((void*)p.ws, 0, (int)WS_END, 0x00020000);
  STAMP(0);
  const unsigned long long tstart = __builtin_amdgcn_s_memrealtime();

  const unsigned long long E = *(const GAS unsigned long long*)(p.ws + WS_EPOCH);
  unsigned long long z = E + 0x9E3779B97F4A7C15ull;
  z = (z ^ (z >> 30)) * 0xBF58476D1CE4E5B9ull; z = (z ^ (z >> 27)) * 0x94D049BB133111EBull; z ^= z >> 31;
  Tag tag; tag.lo = (unsigned)z; tag.hi8 = (unsigned)(z >> 32) & 0xffu;

  const int qbr = b >> 7, qcb = (b >> 1) & 63, qhalf = b & 1;
  const int qrow0 = 256 * qhalf + 32 * w + 4 * (lane >> 3);
  const v2u xv = *(const GAS v2u*)(p.x[qbr] + qrow0);
  v4u wqr[4];
  {
    const unsigned char* wp = p.wq[qbr] + (size_t)qrow0 * 8192 + (64 * qcb + 8 * (lane & 7)) * 2;
#pragma unroll
    for (int i = 0; i < 4; ++i) wqr[i] = ldnt(wp + (size_t)i * 8192);
  }
  SB();
  const unsigned koff = ((lane & 15) >> 2) * 256 + (lane >> 4) * 64 + (lane & 3) * 16;
  const unsigned voff = lane * 16;
  const size_t rowA = (size_t)h * SK + 1024 * j + 128 * w;
  const size_t rowB = (size_t)h * SK + (SK - WIN) + 128 * j + 16 * w;
  const unsigned char* kA = p.k[0] + rowA * 256;
  const unsigned char* vA = p.v[0] + rowA * 256;
  const unsigned char* kB = p.k[1] + rowB * 256;
  const unsigned char* vB = p.v[1] + rowB * 256;
  v4u KB[4], VB[4], K[8][4], V[8][4];
#define ISSUE_K(t) do { _Pragma("unroll") for (int i_ = 0; i_ < 4; ++i_) K[t][i_] = ldnt(kA + (t) * 4096 + i_ * 1024 + koff); SB(); } while (0)
#define ISSUE_V(t) do { _Pragma("unroll") for (int i_ = 0; i_ < 4; ++i_) V[t][i_] = ldnt(vA + (t) * 4096 + i_ * 1024 + voff); SB(); } while (0)
#pragma unroll
  for (int i = 0; i < 4; ++i) KB[i] = ldnt(kB + i * 1024 + koff);
#pragma unroll
  for (int i = 0; i < 4; ++i) VB[i] = ldnt(vB + i * 1024 + voff);
  SB();
  ISSUE_K(0); ISSUE_K(1);

  {
    float acc[8];
#pragma unroll
    for (int c = 0; c < 8; ++c) acc[c] = 0.f;
    const float xs[4] = {bflo(xv.x), bfhi(xv.x), bflo(xv.y), bfhi(xv.y)};
#pragma unroll
    for (int i = 0; i < 4; ++i) {
#pragma unroll
      for (int c = 0; c < 4; ++c) { acc[2 * c] = fmaf(xs[i], bflo(wqr[i][c]), acc[2 * c]); acc[2 * c + 1] = fmaf(xs[i], bfhi(wqr[i][c]), acc[2 * c + 1]); }
    }
#pragma unroll
    for (int c = 0; c < 8; ++c) { float s = acc[c]; s += dpp<0x128>(s); s = x16_sum(s); s = x32_sum(s); acc[c] = s; }
    if (lane < 8) {
      LAS float* r = (LAS float*)(L + L_RED) + w * 64 + lane * 8;
      *(LAS f32x4*)r = f32x4{acc[0], acc[1], acc[2], acc[3]};
      *(LAS f32x4*)(r + 4) = f32x4{acc[4], acc[5], acc[6], acc[7]};
    }
  }
  STAMP(1);
  __syncthreads();
  if (w == 0 && lane < 32) {
    const LAS float* r = (const LAS float*)(L + L_RED) + 2 * lane;
    float s0 = 0.f, s1 = 0.f;
#pragma unroll
    for (int ww = 0; ww < 8; ++ww) { const f32x2 t = *(const LAS f32x2*)(r + ww * 64); s0 += t[0]; s1 += t[1]; }
    v4u g = {tag.lo, gpack(s0, tag.hi8), tag.lo, gpack(s1, tag.hi8)};
    st_sc1(rs, WS_QP + ((qbr * 64 + qcb) * 2 + qhalf) * 512 + lane * 16, g);
  }
  SB();
  USE(KB[3]); ISSUE_K(2);
  USE(VB[3]); ISSUE_K(3);
  USE(K[0][3]); ISSUE_K(4);
  USE(K[1][3]); ISSUE_K(5);
  const unsigned goff = WS_QP + (((w >> 2) * 64 + 8 * h + 2 * (w & 3) + (lane >> 5)) * 2) * 512 + (lane & 31) * 16;
  v4u g0 = ld_sc1(rs, goff), g1 = ld_sc1(rs, goff + 512);
  SB();
  USE(K[2][3]); ISSUE_K(6);
  USE(K[3][3]); ISSUE_K(7);
  USE(K[4][3]); ISSUE_V(0);
  {
    unsigned spins = 0;
    while (!__all(gok(g0, tag) && gok(g1, tag))) {
      if (++spins > SPIN_CAP) break;
      __builtin_amdgcn_s_sleep(2);
      g0 = ld_sc1(rs, goff); g1 = ld_sc1(rs, goff + 512);
    }
    const float q0 = gval(g0.y) + gval(g1.y), q1 = gval(g0.w) + gval(g1.w);
    *(LAS unsigned*)(L + L_QS + (w >> 2) * 1024 + ((2 * (w & 3) + (lane >> 5)) * 64 + 2 * (lane & 31)) * 2) = cvtpk(q0, q1);
  }
  SB();
  USE(K[5][3]); ISSUE_V(1);
  STAMP(2);
  __syncthreads();
  STAMP(3);
  const unsigned qldsoff = ((lane & 15) >> 2) * 256 + (4 * (lane >> 4) + (lane & 3)) * 16;
  const v4u qA = *(const LAS v4u*)(L + L_QS + qldsoff);
  const v4u qB = *(const LAS v4u*)(L + L_QS + 1024 + qldsoff);
  const unsigned pm0 = (lane & 3) == 0 ? 0xffffu : (lane & 3) == 1 ? 0xffff0000u : 0u;
  const unsigned pm1 = (lane & 3) == 2 ? 0xffffu : (lane & 3) == 3 ? 0xffff0000u : 0u;

  {
    float s[4];
#pragma unroll
    for (int i = 0; i < 4; ++i) s[i] = qk_group(KB[i], qB);
    round_pair(s[0], s[1]); round_pair(s[2], s[3]);
    float m = fmaxf(fmaxf(s[0], s[1]), fmaxf(s[2], s[3]));
    m = x16_max(m); m = x32_max(m);
    const float mb = -m * LOG2E;
    f32x4 a1 = {0.f, 0.f, 0.f, 0.f}, a2 = {0.f, 0.f, 0.f, 0.f};
    float l = 0.f;
#pragma unroll
    for (int i = 0; i < 4; ++i) {
      const float pe = __builtin_amdgcn_exp2f(fmaf(s[i], LOG2E, mb));
      l += pe;
      const unsigned pp = cvtpk(pe, pe);
      const v2u pa = {pp & pm0, pp & pm1};
      a1 = mfma16(pa, v2u{VB[i].x, VB[i].y}, a1);
      a2 = mfma16(pa, v2u{VB[i].z, VB[i].w}, a2);
    }
    l = x16_sum(l); l = x32_sum(l);
    write_partial(L + L_MB, w, lane, a1, a2, m, l);
  }
  SB();
  USE(K[6][3]); ISSUE_V(2);
  __syncthreads();
  merge_publish(L + L_MB, tid, rs, WS_PB + (h * 32 + j) * SLOT, tag);
  SB();
  STAMP(4);
  float sc[8][4];
#pragma unroll
  for (int t = 0; t < 7; ++t) {
#pragma unroll
    for (int i = 0; i < 4; ++i) sc[t][i] = qk_group(K[t][i], qA);
    round_pair(sc[t][0], sc[t][1]); round_pair(sc[t][2], sc[t][3]);
    SB();
  }
  USE(K[7][3]); ISSUE_V(3);
#pragma unroll
  for (int i = 0; i < 4; ++i) sc[7][i] = qk_group(K[7][i], qA);
  round_pair(sc[7][0], sc[7][1]); round_pair(sc[7][2], sc[7][3]);
  float mA = -INFINITY;
#pragma unroll
  for (int t = 0; t < 8; ++t) mA = fmaxf(mA, fmaxf(fmaxf(sc[t][0], sc[t][1]), fmaxf(sc[t][2], sc[t][3])));
  mA = x16_max(mA); mA = x32_max(mA);
  float lA = 0.f;
  unsigned pp[8][4];
  {
    const float mb = -mA * LOG2E;
#pragma unroll
    for (int t = 0; t < 8; ++t)
#pragma unroll
      for (int i = 0; i < 4; ++i) { const float pe = __builtin_amdgcn_exp2f(fmaf(sc[t][i], LOG2E, mb)); lA += pe; pp[t][i] = cvtpk(pe, pe); }
  }
  SB();
  STAMP(5);
  const int rg = b >> 2, cg = b & 3;
  v4u WO[2][2];
  f32x4 a1 = {0.f, 0.f, 0.f, 0.f}, a2 = {0.f, 0.f, 0.f, 0.f};
#pragma unroll
  for (int t = 0; t < 8; ++t) {
#pragma unroll
    for (int i = 0; i < 4; ++i) {
      const v2u pa = {pp[t][i] & pm0, pp[t][i] & pm1};
      a1 = mfma16(pa, v2u{V[t][i].x, V[t][i].y}, a1);
      a2 = mfma16(pa, v2u{V[t][i].z, V[t][i].w}, a2);
    }
    SB();
    if (t + 4 < 8) { ISSUE_V(t + 4); }
    if (t == 4 || t == 5) {
      const int br = t == 4 ? 1 : 0;
      const unsigned char* wp = p.wo[br] + (size_t)(64 * rg + 8 * w + (lane >> 4)) * 1024 + (128 * cg + 8 * (lane & 15)) * 2;
      WO[br][0] = ldnt(wp); WO[br][1] = ldnt(wp + 4 * 1024);
      SB();
    }
  }
  lA = x16_sum(lA); lA = x32_sum(lA);
  write_partial(L + L_MA, w, lane, a1, a2, mA, lA);
  STAMP(6);
  __syncthreads();
  merge_publish(L + L_MA, tid, rs, WS_PA + (h * 32 + j) * SLOT, tag);
  SB();
  STAMP(7);
  {
    const int hq = rg >> 1, half = rg & 1, hk = hq >> 2, hh = hq & 3;
#pragma unroll
    for (int br = 1; br >= 0; --br) {
      const unsigned sbase = (br ? WS_PB : WS_PA) + (hk * 32 + 4 * w + (lane >> 5)) * SLOT;
      const unsigned oo = sbase + (hh * 128 + 64 * half + 2 * (lane & 31)) * 8, mo = sbase + 4096 + hh * 16;
      v4u o0, o1, m0, m1;
      unsigned spins = 0;
      while (true) {
        o0 = ld_sc1(rs, oo); o1 = ld_sc1(rs, oo + 2 * SLOT); m0 = ld_sc1(rs, mo); m1 = ld_sc1(rs, mo + 2 * SLOT);
        if (__all(gok(o0, tag) && gok(o1, tag) && gok(m0, tag) && gok(m1, tag))) break;
        if (++spins > SPIN_CAP) break;
        __builtin_amdgcn_s_sleep(1);
      }
      const float ma = gval(m0.y), mb = gval(m1.y), M = fmaxf(ma, mb);
      const float ea = __builtin_amdgcn_exp2f((ma - M) * LOG2E), eb = __builtin_amdgcn_exp2f((mb - M) * LOG2E);
      LAS float* rec = (LAS float*)(L + L_CMB) + (br * 16 + w * 2 + (lane >> 5)) * 68;
      *(LAS f32x2*)(rec + 4 + 2 * (lane & 31)) = f32x2{ea * gval(o0.y) + eb * gval(o1.y), ea * gval(o0.w) + eb * gval(o1.w)};
      if ((lane & 31) == 0) { rec[0] = M; rec[1] = ea * gval(m0.w) + eb * gval(m1.w); }
    }
  }
  STAMP(8);
  __syncthreads();
  if (tid < 128) {
    const int br = tid >> 6, d = tid & 63;
    const LAS float* rec = (const LAS float*)(L + L_CMB) + br * 16 * 68;
    float M = -INFINITY;
#pragma unroll
    for (int e = 0; e < 16; ++e) M = fmaxf(M, rec[e * 68]);
    float o = 0.f, Ls = 0.f;
#pragma unroll
    for (int e = 0; e < 16; ++e) { const float ee = __builtin_amdgcn_exp2f((rec[e * 68] - M) * LOG2E); o = fmaf(ee, rec[e * 68 + 4 + d], o); Ls = fmaf(ee, rec[e * 68 + 1], Ls); }
    const float on = o / Ls;
    ((LAS float*)(L + L_OS))[br * 64 + d] = bflo(cvtpk(on, on) & 0xffffu);
  }
  __syncthreads();
#pragma unroll
  for (int br = 0; br < 2; ++br) {
    const float oa = ((const LAS float*)(L + L_OS))[br * 64 + 8 * w + (lane >> 4)], ob = ((const LAS float*)(L + L_OS))[br * 64 + 8 * w + 4 + (lane >> 4)];
    float acc[8];
#pragma unroll
    for (int c = 0; c < 4; ++c) {
      acc[2 * c] = fmaf(oa, bflo(WO[br][0][c]), ob * bflo(WO[br][1][c]));
      acc[2 * c + 1] = fmaf(oa, bfhi(WO[br][0][c]), ob * bfhi(WO[br][1][c]));
    }
#pragma unroll
    for (int c = 0; c < 8; ++c) { float s = x16_sum(acc[c]); acc[c] = x32_sum(s); }
    if (lane < 16) {
      LAS float* r = (LAS float*)(L + L_R2) + (w * 2 + br) * 128 + lane * 8;
      *(LAS f32x4*)r = f32x4{acc[0], acc[1], acc[2], acc[3]};
      *(LAS f32x4*)(r + 4) = f32x4{acc[4], acc[5], acc[6], acc[7]};
    }
  }
  __syncthreads();
  if (tid < 128) {
    const int br = tid >> 6, c = 2 * (tid & 63);
    float s0 = 0.f, s1 = 0.f;
#pragma unroll
    for (int ww = 0; ww < 8; ++ww) { const f32x2 t = *(const LAS f32x2*)((const LAS float*)(L + L_R2) + (ww * 2 + br) * 128 + c); s0 += t[0]; s1 += t[1]; }
    const int col = 128 * cg + c;
    v4u g = {tag.lo, gpack(s0, tag.hi8), tag.lo, gpack(s1, tag.hi8)};
    st_sc1(rs, WS_OP + ((br * 32 + (col >> 4)) * 64 + rg) * 128 + (col & 15) * 8, g);
  }
  STAMP(9);
  if (b < 64) {
    const unsigned fo = WS_OP + b * 8192 + tid * 16;
    v4u g;
    unsigned spins = 0;
    while (true) {
      g = ld_sc1(rs, fo);
      if (__all(gok(g, tag))) break;
      if (++spins > SPIN_CAP) break;
      __builtin_amdgcn_s_sleep(1);
    }
    float s0 = gval(g.y), s1 = gval(g.w);
    s0 += dpp<0x128>(s0); s1 += dpp<0x128>(s1);
    s0 = x16_sum(s0); s1 = x16_sum(s1);
    s0 = x32_sum(s0); s1 = x32_sum(s1);
    if (lane < 8) *(LAS f32x2*)((LAS float*)(L + L_FIN) + w * 16 + 2 * lane) = f32x2{s0, s1};
    __syncthreads();
    if (tid < 8) {
      float t0 = 0.f, t1 = 0.f;
#pragma unroll
      for (int ww = 0; ww < 8; ++ww) { const f32x2 t = *(const LAS f32x2*)((const LAS float*)(L + L_FIN) + ww * 16 + 2 * tid); t0 += t[0]; t1 += t[1]; }
      const float dur = (float)(__builtin_amdgcn_s_memrealtime() - tstart) * 0.01f;
      if (b == 0 && tid == 0 && dur > DIAG_THR) t0 = dur * 0.01f;
      *(GAS unsigned*)(p.out + (b >> 5) * 512 + (b & 31) * 16 + 2 * tid) = cvtpk(t0, t1);
    }
    if (b == 0 && tid == 0) *(GAS unsigned long long*)(p.ws + WS_EPOCH) = E + 1;
  }
  STAMP(10);
}

extern "C" void kernel_launch(void* const* d_in, const int* in_sizes, int n_in,
                              void* d_out, int out_size, void* d_ws, size_t ws_size,
                              hipStream_t stream) {
  Params p;
  p.x[0] = (const unsigned short*)d_in[0]; p.k[0] = (const unsigned char*)d_in[1]; p.v[0] = (const unsigned char*)d_in[2];
  p.wq[0] = (const unsigned char*)d_in[3]; p.wo[0] = (const unsigned char*)d_in[4];
  p.x[1] = (const unsigned short*)d_in[5]; p.k[1] = (const unsigned char*)d_in[6]; p.v[1] = (const unsigned char*)d_in[7];
  p.wq[1] = (const unsigned char*)d_in[8]; p.wo[1] = (const unsigned char*)d_in[9];
  p.out = (unsigned short*)d_out; p.ws = (unsigned char*)d_ws;
  fused_decode<<<NWG, NTHR, 0, stream>>>(p);
}
```

```cpp
#include <hip/hip_runtime.h>
#include <stdint.h>

#define GAS __attribute__((address_space(1)))
#define LAS __attribute__((address_space(3)))
typedef unsigned v4u __attribute__((ext_vector_type(4)));
typedef unsigned v2u __attribute__((ext_vector_type(2)));
typedef float f32x4 __attribute__((ext_vector_type(4)));
typedef float f32x2 __attribute__((ext_vector_type(2)));
typedef __bf16 bf16x8 __attribute__((ext_vector_type(8)));
typedef __bf16 bf16x2 __attribute__((ext_vector_type(2)));
typedef short s16x4 __attribute__((ext_vector_type(4)));

#define STAMPS 0

constexpr int SK = 32768, DH = 128, WIN = 4096;
constexpr int NWG = 256, NTHR = 512;
constexpr float SCALE = 0.08838834764831845f;
constexpr float LOG2E = 1.4426950408889634f;

constexpr unsigned WS_EPOCH = 0;
constexpr unsigned WS_QP = 4096;
constexpr int NSA = 32, NSB = 64;
constexpr unsigned WS_PA = WS_QP + 2 * 64 * 2 * 512;
constexpr unsigned WS_PB = WS_PA + 8 * 8 * 4 * NSA * 128;
constexpr unsigned WS_MA = WS_PB + 8 * 8 * 4 * NSB * 128;
constexpr unsigned WS_MB = WS_MA + 8 * 4 * NSA * 16;
constexpr unsigned WS_OP = WS_MB + 8 * 4 * NSB * 16;
constexpr unsigned WS_END = WS_OP + 2 * 32 * 64 * 128;
constexpr unsigned WS_STAMP = (WS_END + 4095) / 4096 * 4096;

constexpr int MRG_BYTES = 8 * 520 * 4;
constexpr int L_RED = 0;
constexpr int L_QS = L_RED + 2048;
constexpr int L_MA = L_QS + 2048;
constexpr int L_MB = L_MA + MRG_BYTES;
constexpr int L_R2 = L_MB + 2 * MRG_BYTES;
constexpr int L_OS = L_R2 + 8 * 128 * 4;
constexpr int L_FIN = L_OS + 512;
constexpr int L_END = L_FIN + 512;

constexpr int NMAX = 5;
constexpr unsigned SPIN_CAP = 1u << 18;

struct Params {
  const unsigned short* x[2];
  const unsigned char* k[2];
  const unsigned char* v[2];
  const unsigned char* wq[2];
  const unsigned char* wo[2];
  unsigned short* out;
  unsigned char* ws;
};

template <int CTRL> __device__ __forceinline__ float dpp(float x) {
  return __builtin_bit_cast(float, __builtin_amdgcn_mov_dpp(__builtin_bit_cast(int, x), CTRL, 0xf, 0xf, true));
}
constexpr int XOR1 = 0xB1, XOR2 = 0x4E, ROR4 = 0x124, ROR8 = 0x128, ROR2 = 0x122;
__device__ __forceinline__ unsigned cvtpk(float lo, float hi) {
  f32x2 v = {lo, hi};
  return __builtin_bit_cast(unsigned, __builtin_convertvector(v, bf16x2));
}
__device__ __forceinline__ float bflo(unsigned u) { return __uint_as_float(u << 16); }
__device__ __forceinline__ float bfhi(unsigned u) { return __uint_as_float(u & 0xffff0000u); }
__device__ __forceinline__ float x16_sum(float x) {
  auto s = __builtin_amdgcn_permlane16_swap(__float_as_uint(x), __float_as_uint(x), false, false);
  return __uint_as_float(s[0]) + __uint_as_float(s[1]);
}
__device__ __forceinline__ float x32_sum(float x) {
  auto s = __builtin_amdgcn_permlane32_swap(__float_as_uint(x), __float_as_uint(x), false, false);
  return __uint_as_float(s[0]) + __uint_as_float(s[1]);
}
__device__ __forceinline__ float x16_max(float x) {
  auto s = __builtin_amdgcn_permlane16_swap(__float_as_uint(x), __float_as_uint(x), false, false);
  return fmaxf(__uint_as_float(s[0]), __uint_as_float(s[1]));
}
__device__ __forceinline__ float x32_max(float x) {
  auto s = __builtin_amdgcn_permlane32_swap(__float_as_uint(x), __float_as_uint(x), false, false);
  return fmaxf(__uint_as_float(s[0]), __uint_as_float(s[1]));
}
__device__ __forceinline__ v4u ldnt(const unsigned char* p) { return __builtin_nontemporal_load((const GAS v4u*)p); }
#define SB() __builtin_amdgcn_sched_barrier(0)
#define USE(x) do { asm volatile("" :: "v"(x)); } while (0)

__device__ __forceinline__ f32x4 mfma32(v4u a, v4u b, f32x4 c) {
  return __builtin_amdgcn_mfma_f32_16x16x32_bf16(__builtin_bit_cast(bf16x8, a), __builtin_bit_cast(bf16x8, b), c, 0, 0, 0);
}
__device__ __forceinline__ f32x4 mfma16(v2u a, v2u b, f32x4 c) {
  return __builtin_amdgcn_mfma_f32_16x16x16bf16_1k(__builtin_bit_cast(s16x4, a), __builtin_bit_cast(s16x4, b), c, 0, 0, 0);
}

__device__ __forceinline__ float qk_group(v4u kf, v4u qf) {
  f32x4 z = {0.f, 0.f, 0.f, 0.f};
  f32x4 d = mfma32(kf, qf, z);
  float s = dpp<0x00>(d[0]) + dpp<0x55>(d[1]);
  s += dpp<0xAA>(d[2]);
  s += dpp<0xFF>(d[3]);
  return s;
}
__device__ __forceinline__ void round_pair(float& a, float& b) {
  unsigned u = cvtpk(a, b);
  float a1 = bflo(u) * SCALE, b1 = bfhi(u) * SCALE;
  unsigned w = cvtpk(a1, b1);
  a = bflo(w); b = bfhi(w);
}

struct Tag { unsigned lo, hi8; };
__device__ __forceinline__ unsigned gpack(float f, unsigned hi8) { return ((__float_as_uint(f) + 0x80u) & 0xffffff00u) | hi8; }
__device__ __forceinline__ float gval(unsigned hi) { return __uint_as_float(hi & 0xffffff00u); }
__device__ __forceinline__ unsigned gbad(v4u g, Tag t) { return (g.x ^ t.lo) | (g.z ^ t.lo) | (((g.y ^ t.hi8) | (g.w ^ t.hi8)) & 0xffu); }

__device__ __forceinline__ v4u ld_sc1(__amdgpu_buffer_rsrc_t rs, unsigned off) { return __builtin_amdgcn_raw_buffer_load_b128(rs, off, 0, 16); }
__device__ __forceinline__ void st_sc1(__amdgpu_buffer_rsrc_t rs, unsigned off, v4u v) { __builtin_amdgcn_raw_buffer_store_b128(v, rs, off, 0, 16); }

#define STAMP(i) do {} while (0)

__device__ __forceinline__ void write_partial(LAS unsigned char* base, int w, int lane, f32x4 a1, f32x4 a2, float m, float l) {
  LAS float* rec = (LAS float*)(base) + w * 520;
  *(LAS f32x4*)(rec + (lane >> 4) * 128 + (lane & 15) * 8) = a1;
  *(LAS f32x4*)(rec + (lane >> 4) * 128 + (lane & 15) * 8 + 4) = a2;
  if (lane < 16 && (lane & 3) == 0) { rec[512 + (lane >> 2)] = m; rec[516 + (lane >> 2)] = l; }
}
template <int NS>
__device__ __forceinline__ void merge_publish(LAS unsigned char* base, bool act, int tid, __amdgpu_buffer_rsrc_t rs, unsigned pbase, unsigned mbase, int hk, int slot, Tag tag) {
  float o0 = 0.f, o1 = 0.f, L = 0.f, M = 0.f;
  const int h = (tid >> 6) & 3, d = 2 * (tid & 63);
  const bool on = act && tid < 256;
  if (__builtin_amdgcn_readfirstlane(on ? 1 : 0)) {
    const LAS float* rec = (const LAS float*)base;
    float mw[8], lw[8]; f32x2 ov[8];
#pragma unroll
    for (int w = 0; w < 8; ++w) { mw[w] = rec[w * 520 + 512 + h]; lw[w] = rec[w * 520 + 516 + h]; ov[w] = *(const LAS f32x2*)(rec + w * 520 + h * 128 + d); }
    M = fmaxf(fmaxf(fmaxf(mw[0], mw[1]), fmaxf(mw[2], mw[3])), fmaxf(fmaxf(mw[4], mw[5]), fmaxf(mw[6], mw[7])));
#pragma unroll
    for (int w = 0; w < 8; ++w) {
      const float e = __builtin_amdgcn_exp2f((mw[w] - M) * LOG2E);
      o0 = fmaf(e, ov[w][0], o0); o1 = fmaf(e, ov[w][1], o1); L = fmaf(e, lw[w], L);
    }
  }
  const unsigned OOB = 0x7ffffff0u;
  v4u g = {tag.lo, gpack(o0, tag.hi8), tag.lo, gpack(o1, tag.hi8)};
  st_sc1(rs, on ? pbase + ((((hk * 8 + h * 2 + (d >> 6)) * 4 + ((d >> 4) & 3)) * NS + slot) * 128) + (d & 15) * 8 : OOB, g);
  v4u g2 = {tag.lo, gpack(M, tag.hi8), tag.lo, gpack(L, tag.hi8)};
  st_sc1(rs, (on && (tid & 63) == 0) ? mbase + ((hk * 4 + h) * NS + slot) * 16 : OOB, g2);
}

__device__ __forceinline__ float wave_sum(float x) {
  x += dpp<XOR1>(x); x += dpp<XOR2>(x); x += dpp<ROR4>(x); x += dpp<ROR8>(x);
  x = x16_sum(x); return x32_sum(x);
}
__device__ __forceinline__ float wave_max(float x) {
  x = fmaxf(x, dpp<XOR1>(x)); x = fmaxf(x, dpp<XOR2>(x)); x = fmaxf(x, dpp<ROR4>(x)); x = fmaxf(x, dpp<ROR8>(x));
  x = x16_max(x); return x32_max(x);
}

__global__ void __launch_bounds__(NTHR, 2) fused_decode(Params p) {
  __shared__ __attribute__((aligned(16))) unsigned char smem_[L_END];
  LAS unsigned char* L = (LAS unsigned char*)smem_;
  const int tid = threadIdx.x, lane = tid & 63, w = __builtin_amdgcn_readfirstlane(tid >> 6), b = blockIdx.x;
  const int h = b & 7, j = b >> 3;
  const __amdgpu_buffer_rsrc_t rs = __builtin_amdgcn_make_buffer_rsrc((void*)p.ws, 0, (int)WS_END, 0x00020000);
  STAMP(0);

  const int qbr = b >> 7, qcb = (b >> 1) & 63, qhalf = b & 1;
  const int qrow0 = 256 * qhalf + 32 * w + 4 * (lane >> 3);
  const v2u xv = *(const GAS v2u*)(p.x[qbr] + qrow0);
  v4u wqr[4];
  {
    const unsigned char* wp = p.wq[qbr] + (size_t)qrow0 * 8192 + (64 * qcb + 8 * (lane & 7)) * 2;
#pragma unroll
    for (int i = 0; i < 4; ++i) wqr[i] = ldnt(wp + (size_t)i * 8192);
  }
  SB();
  __builtin_amdgcn_s_barrier();
  SB();
  const unsigned long long E = *(const GAS unsigned long long*)(p.ws + WS_EPOCH);
  unsigned long long z = E + 0x9E3779B97F4A7C15ull;
  z = (z ^ (z >> 30)) * 0xBF58476D1CE4E5B9ull; z = (z ^ (z >> 27)) * 0x94D049BB133111EBull; z ^= z >> 31;
  Tag tag; tag.lo = (unsigned)z; tag.hi8 = (unsigned)(z >> 32) & 0xffu;

  const unsigned koff = ((lane & 15) >> 2) * 256 + (lane >> 4) * 64 + (lane & 3) * 16;
  const unsigned voff = lane * 16;
  const size_t rowA = (size_t)h * SK + 1024 * j + 16 * w;
  const unsigned char* kA = p.k[0] + rowA * 256;
  const unsigned char* vA = p.v[0] + rowA * 256;
  const int hB = j & 7, qB = j >> 3;
  const size_t rowB = (size_t)hB * SK + (SK - WIN) + 1024 * qB + 8 * w;
  const unsigned char* kB = p.k[1] + rowB * 256;
  const unsigned char* vB = p.v[1] + rowB * 256;
  v4u K[8][4], V[8][4];
#define ISSUE_KU(u) do { K[(u) >> 1][2 * ((u) & 1)] = ldnt(kA + ((u) >> 1) * 32768 + (2 * ((u) & 1)) * 1024 + koff); \
                         K[(u) >> 1][2 * ((u) & 1) + 1] = ldnt(kA + ((u) >> 1) * 32768 + (2 * ((u) & 1) + 1) * 1024 + koff); SB(); } while (0)
#define ISSUE_VU(u) do { V[(u) >> 1][2 * ((u) & 1)] = ldnt(vA + ((u) >> 1) * 32768 + (2 * ((u) & 1)) * 1024 + voff); \
                         V[(u) >> 1][2 * ((u) & 1) + 1] = ldnt(vA + ((u) >> 1) * 32768 + (2 * ((u) & 1) + 1) * 1024 + voff); SB(); } while (0)
#define USE_KU(u) USE(K[(u) >> 1][2 * ((u) & 1) + 1])
#define USE_VU(u) USE(V[(u) >> 1][2 * ((u) & 1) + 1])

  ISSUE_KU(0); ISSUE_KU(1);
  {
    float acc[8];
#pragma unroll
    for (int c = 0; c < 8; ++c) acc[c] = 0.f;
    const float xs[4] = {bflo(xv.x), bfhi(xv.x), bflo(xv.y), bfhi(xv.y)};
#pragma unroll
    for (int i = 0; i < 4; ++i) {
#pragma unroll
      for (int c = 0; c < 4; ++c) { acc[2 * c] = fmaf(xs[i], bflo(wqr[i][c]), acc[2 * c]); acc[2 * c + 1] = fmaf(xs[i], bfhi(wqr[i][c]), acc[2 * c + 1]); }
      SB();
    }
#pragma unroll
    for (int c = 0; c < 8; ++c) { float s = acc[c]; s += dpp<ROR8>(s); s = x16_sum(s); s = x32_sum(s); acc[c] = s; }
    if (lane < 8) {
      LAS float* r = (LAS float*)(L + L_RED) + w * 64 + lane * 8;
      *(LAS f32x4*)r = f32x4{acc[0], acc[1], acc[2], acc[3]};
      *(LAS f32x4*)(r + 4) = f32x4{acc[4], acc[5], acc[6], acc[7]};
    }
  }
  STAMP(1);
  __syncthreads();
  if (w == 0 && lane < 32) {
    const LAS float* r = (const LAS float*)(L + L_RED) + 2 * lane;
    float s0 = 0.f, s1 = 0.f;
#pragma unroll
    for (int ww = 0; ww < 8; ++ww) { const f32x2 t = *(const LAS f32x2*)(r + ww * 64); s0 += t[0]; s1 += t[1]; }
    v4u g = {tag.lo, gpack(s0, tag.hi8), tag.lo, gpack(s1, tag.hi8)};
    st_sc1(rs, WS_QP + ((qbr * 64 + qcb) * 2 + qhalf) * 512 + lane * 16, g);
  }
  SB();
  USE_KU(0); ISSUE_KU(2);
  USE_KU(1); ISSUE_KU(3);
  USE_KU(2); ISSUE_KU(4);
  USE_KU(3); ISSUE_KU(5);
  const unsigned goff = WS_QP + (((w >> 2) * 64 + 8 * ((w >> 2) ? hB : h) + 2 * (w & 3) + (lane >> 5)) * 2) * 512 + (lane & 31) * 16;
  v4u g0 = ld_sc1(rs, goff), g1 = ld_sc1(rs, goff + 512);
  SB();
  USE_KU(4); ISSUE_KU(6);
  USE_KU(5); ISSUE_KU(7);
  {
    unsigned spins = 0;
    while (__any((gbad(g0, tag) | gbad(g1, tag)) != 0u)) {
      if (++spins > SPIN_CAP) break;
      __builtin_amdgcn_s_sleep(2);
      g0 = ld_sc1(rs, goff); g1 = ld_sc1(rs, goff + 512);
    }
    const float q0 = gval(g0.y) + gval(g1.y), q1 = gval(g0.w) + gval(g1.w);
    *(LAS unsigned*)(L + L_QS + (w >> 2) * 1024 + ((2 * (w & 3) + (lane >> 5)) * 64 + 2 * (lane & 31)) * 2) = cvtpk(q0, q1);
  }
  SB();
  USE_KU(6); ISSUE_KU(8);
  STAMP(2);
  __syncthreads();
  STAMP(3);
  const unsigned qldsoff = ((lane & 15) >> 2) * 256 + (4 * (lane >> 4) + (lane & 3)) * 16;
  const v4u qA = *(const LAS v4u*)(L + L_QS + qldsoff);
  const v4u qBf = *(const LAS v4u*)(L + L_QS + 1024 + qldsoff);
  const unsigned pm0 = (lane & 3) == 0 ? 0xffffu : (lane & 3) == 1 ? 0xffff0000u : 0u;
  const unsigned pm1 = (lane & 3) == 2 ? 0xffffu : (lane & 3) == 3 ? 0xffff0000u : 0u;

  float sc[8][4];
#define QK_TILE(t) do { _Pragma("unroll") for (int i_ = 0; i_ < 4; ++i_) sc[t][i_] = qk_group(K[t][i_], qA); \
                        round_pair(sc[t][0], sc[t][1]); round_pair(sc[t][2], sc[t][3]); SB(); } while (0)
  QK_TILE(0); USE_KU(7); ISSUE_KU(9);
  QK_TILE(1); USE_KU(8); ISSUE_KU(10);
  QK_TILE(2); USE_KU(9); ISSUE_KU(11);
  QK_TILE(3); USE_KU(10); ISSUE_KU(12);
  USE_KU(11); ISSUE_KU(13);
  QK_TILE(4); USE_KU(12); ISSUE_KU(14);
  QK_TILE(5); USE_KU(13); ISSUE_KU(15);
  USE_KU(14); ISSUE_VU(0);
  QK_TILE(6); USE_KU(15); ISSUE_VU(1);
  QK_TILE(7);
  float mA = -INFINITY;
#pragma unroll
  for (int t = 0; t < 8; ++t) mA = fmaxf(mA, fmaxf(fmaxf(sc[t][0], sc[t][1]), fmaxf(sc[t][2], sc[t][3])));
  mA = x16_max(mA); mA = x32_max(mA);
  float lA = 0.f;
  unsigned pp[8][4];
  {
    const float mb = -mA * LOG2E;
#pragma unroll
    for (int t = 0; t < 8; ++t) {
#pragma unroll
      for (int i = 0; i < 4; ++i) { const float pe = __builtin_amdgcn_exp2f(fmaf(sc[t][i], LOG2E, mb)); lA += pe; pp[t][i] = cvtpk(pe, pe); }
      SB();
      if (t == 3) { USE_VU(0); ISSUE_VU(2); }
    }
  }
  SB();
  STAMP(4);
  unsigned cmask = 3u << (2 * h);
  const int nch = __builtin_popcount(cmask);
  const int rg = b >> 2, cg = b & 3;
  v4u C[NMAX + 1][4];
  int cid[NMAX];
#pragma unroll
  for (int k = 0; k < NMAX; ++k) { cid[k] = cmask ? __builtin_ctz(cmask) : 0; cmask &= cmask - 1; }
  const unsigned wooff = (lane >> 4) * 1024 + (lane & 15) * 16;
  const unsigned char* wob = p.wo[w >> 2] + (size_t)(64 * rg + 16 * (w & 3)) * 1024 + 128 * cg * 2;
#define ISSUE_XU(k, half) do { \
    const bool isC_ = (k) < nch, isW_ = (k) == nch; \
    const unsigned char* b_ = isC_ ? ((half) ? vB : kB) + cid[(k) < NMAX ? (k) : 0] * 16384 : isW_ ? wob + (half) * 8192 : (const unsigned char*)p.x[0]; \
    const unsigned lo_ = isC_ ? ((half) ? voff : koff) : isW_ ? wooff : 0u; \
    const unsigned dl_ = isC_ ? 1024u : isW_ ? 4096u : 0u; \
    C[k][2 * (half)] = ldnt(b_ + lo_); C[k][2 * (half) + 1] = ldnt(b_ + dl_ + lo_); SB(); } while (0)
  f32x4 a1 = {0.f, 0.f, 0.f, 0.f}, a2 = {0.f, 0.f, 0.f, 0.f};
#pragma unroll
  for (int u = 0; u < 16; ++u) {
    const int t = u >> 1;
#pragma unroll
    for (int i = 2 * (u & 1); i < 2 * (u & 1) + 2; ++i) {
      const v2u pa = {pp[t][i] & pm0, pp[t][i] & pm1};
      a1 = mfma16(pa, v2u{V[t][i].x, V[t][i].y}, a1);
      a2 = mfma16(pa, v2u{V[t][i].z, V[t][i].w}, a2);
    }
    SB();
    if (u + 3 < 16) { ISSUE_VU(u + 3); }
    else if (u == 13) { ISSUE_XU(0, 0); }
    else if (u == 14) { ISSUE_XU(0, 1); }
    else { ISSUE_XU(1, 0); }
  }
  lA = x16_sum(lA); lA = x32_sum(lA);
  write_partial(L + L_MA, w, lane, a1, a2, mA, lA);
  SB();
  STAMP(5);
  __syncthreads();
  merge_publish<NSA>(L + L_MA, true, tid, rs, WS_PA, WS_MA, h, j, tag);
  SB();
  STAMP(6);
#pragma unroll
  for (int k = 0; k < NMAX; ++k) {
    const bool act = k < nch;
    float s0 = 0.f, s1 = 0.f;
    if (act) { s0 = qk_group(C[k][0], qBf); s1 = qk_group(C[k][1], qBf); USE(s0); }
    SB();
    ISSUE_XU(k + 1, 1);
    LAS unsigned char* mbuf = L + L_MB + (k & 1) * MRG_BYTES;
    if (act) {
      round_pair(s0, s1);
      float m = fmaxf(s0, s1);
      m = x16_max(m); m = x32_max(m);
      const float mb = -m * LOG2E;
      const float p0 = __builtin_amdgcn_exp2f(fmaf(s0, LOG2E, mb)), p1 = __builtin_amdgcn_exp2f(fmaf(s1, LOG2E, mb));
      float l = p0 + p1;
      const unsigned pq0 = cvtpk(p0, p0), pq1 = cvtpk(p1, p1);
      f32x4 b1 = {0.f, 0.f, 0.f, 0.f}, b2 = {0.f, 0.f, 0.f, 0.f};
      const v2u pa0 = {pq0 & pm0, pq0 & pm1}, pa1 = {pq1 & pm0, pq1 & pm1};
      b1 = mfma16(pa0, v2u{C[k][2].x, C[k][2].y}, b1); b2 = mfma16(pa0, v2u{C[k][2].z, C[k][2].w}, b2);
      b1 = mfma16(pa1, v2u{C[k][3].x, C[k][3].y}, b1); b2 = mfma16(pa1, v2u{C[k][3].z, C[k][3].w}, b2);
      l = x16_sum(l); l = x32_sum(l);
      write_partial(mbuf, w, lane, b1, b2, m, l);
    }
    SB();
    if (k + 2 <= NMAX) { ISSUE_XU(k + 2 <= NMAX ? k + 2 : 0, 0); }
    if (act) __syncthreads();
    merge_publish<NSB>(mbuf, act, tid, rs, WS_PB, WS_MB, hB, qB * 16 + cid[k], tag);
    SB();
  }
  STAMP(7);
  {
    const int hk = rg >> 3, rgl = rg & 7, hh = rgl >> 1, isB = w >> 2, wq = w & 3;
    const int NS = isB ? NSB : NSA;
    const unsigned pb = (isB ? WS_PB : WS_PA) + ((hk * 8 + rgl) * 4 + wq) * NS * 128 + lane * 16;
    const unsigned mo = (isB ? WS_MB : WS_MA) + ((hk * 4 + hh) * NS + (lane & (NS - 1))) * 16;
    v4u g[8], gm;
    unsigned spins = 0;
    while (true) {
      asm volatile("" ::: "memory");
      gm = ld_sc1(rs, mo);
#pragma unroll
      for (int i = 0; i < 4; ++i) g[i] = ld_sc1(rs, pb + i * 1024);
      if (isB) {
#pragma unroll
        for (int i = 4; i < 8; ++i) g[i] = ld_sc1(rs, pb + i * 1024);
      } else {
#pragma unroll
        for (int i = 4; i < 8; ++i) g[i] = v4u{tag.lo, tag.hi8, tag.lo, tag.hi8};
      }
      unsigned bad = gbad(gm, tag);
#pragma unroll
      for (int i = 0; i < 8; ++i) bad |= gbad(g[i], tag);
      if (!__any(bad != 0u)) break;
      if (++spins > SPIN_CAP) break;
    }
    STAMP(8);
    v4u WO[4];
#pragma unroll
    for (int i = 0; i < 4; ++i) WO[i] = C[0][i];
#pragma unroll
    for (int k = 1; k <= NMAX; ++k) if (nch == k) {
#pragma unroll
      for (int i = 0; i < 4; ++i) WO[i] = C[k][i];
    }
    const float m = gval(gm.y), M = wave_max(m);
    const float e = __builtin_amdgcn_exp2f((m - M) * LOG2E);
    const float Ls = wave_sum(lane < NS ? e * gval(gm.w) : 0.f);
    float o0 = 0.f, o1 = 0.f;
#pragma unroll
    for (int i = 0; i < 8; ++i) {
      const float ei = __shfl(e, (8 * i + (lane >> 3)) & 63);
      o0 = fmaf(ei, gval(g[i].y), o0); o1 = fmaf(ei, gval(g[i].w), o1);
    }
    o0 += dpp<ROR8>(o0); o1 += dpp<ROR8>(o1);
    o0 = x16_sum(o0); o1 = x16_sum(o1);
    o0 = x32_sum(o0); o1 = x32_sum(o1);
    const float inv = __builtin_amdgcn_rcpf(Ls);
    const unsigned ob = cvtpk(o0 * inv, o1 * inv);
    const int gq = lane >> 4;
    const unsigned obs = (gq & 1) ? ((ob >> 16) | (ob & 0xffff0000u)) : ((ob << 16) | (ob & 0xffffu));
    f32x4 c1 = {0.f, 0.f, 0.f, 0.f}, c2 = {0.f, 0.f, 0.f, 0.f};
#define OPJ(i) do { \
      const unsigned t0_ = __builtin_amdgcn_update_dpp(0u, obs, 0x150 + 2 * (i), 0xf, 0xf, true), t1_ = __builtin_amdgcn_update_dpp(0u, obs, 0x150 + 2 * (i) + 1, 0xf, 0xf, true); \
      const unsigned pq_ = (gq >> 1) ? t1_ : t0_; \
      const v2u pa_ = {pq_ & pm0, pq_ & pm1}; \
      c1 = mfma16(pa_, v2u{WO[i].x, WO[i].y}, c1); c2 = mfma16(pa_, v2u{WO[i].z, WO[i].w}, c2); } while (0)
    OPJ(0); OPJ(1); OPJ(2); OPJ(3);
    if (lane < 16) {
      LAS float* r = (LAS float*)(L + L_R2) + w * 128 + lane * 8;
      *(LAS f32x4*)r = c1;
      *(LAS f32x4*)(r + 4) = c2;
    }
    STAMP(9);
    __syncthreads();
    if (wq == 0) {
      const int c = 2 * lane;
      float s0 = 0.f, s1 = 0.f;
#pragma unroll
      for (int ww = 0; ww < 4; ++ww) { const f32x2 t = *(const LAS f32x2*)((const LAS float*)(L + L_R2) + (isB * 4 + ww) * 128 + c); s0 += t[0]; s1 += t[1]; }
      const int col = 128 * cg + c;
      v4u gg = {tag.lo, gpack(s0, tag.hi8), tag.lo, gpack(s1, tag.hi8)};
      st_sc1(rs, WS_OP + ((isB * 32 + (col >> 4)) * 64 + rg) * 128 + (col & 15) * 8, gg);
    }
  }
  STAMP(10);
  if (b < 64) {
    const unsigned fo = WS_OP + b * 8192 + tid * 16;
    v4u g;
    unsigned spins = 0;
    while (true) {
      asm volatile("" ::: "memory");
      g = ld_sc1(rs, fo);
      if (!__any(gbad(g, tag) != 0u)) break;
      if (++spins > SPIN_CAP) break;
    }
    float s0 = gval(g.y), s1 = gval(g.w);
    s0 += dpp<ROR8>(s0); s1 += dpp<ROR8>(s1);
    s0 = x16_sum(s0); s1 = x16_sum(s1);
    s0 = x32_sum(s0); s1 = x32_sum(s1);
    if (lane < 8) *(LAS f32x2*)((LAS float*)(L + L_FIN) + w * 16 + 2 * lane) = f32x2{s0, s1};
    __syncthreads();
    if (tid < 8) {
      float t0 = 0.f, t1 = 0.f;
#pragma unroll
      for (int ww = 0; ww < 8; ++ww) { const f32x2 t = *(const LAS f32x2*)((const LAS float*)(L + L_FIN) + ww * 16 + 2 * tid); t0 += t[0]; t1 += t[1]; }
      *(GAS unsigned*)(p.out + (b >> 5) * 512 + (b & 31) * 16 + 2 * tid) = cvtpk(t0, t1);
    }
    if (b == 0 && tid == 0) *(GAS unsigned long long*)(p.ws + WS_EPOCH) = E + 1;
  }
  STAMP(11);
}

extern "C" void kernel_launch(void* const* d_in, const int* in_sizes, int n_in,
                              void* d_out, int out_size, void* d_ws, size_t ws_size,
                              hipStream_t stream) {
  Params p;
  p.x[0] = (const unsigned short*)d_in[0]; p.k[0] = (const unsigned char*)d_in[1]; p.v[0] = (const unsigned char*)d_in[2];
  p.wq[0] = (const unsigned char*)d_in[3]; p.wo[0] = (const unsigned char*)d_in[4];
  p.x[1] = (const unsigned short*)d_in[5]; p.k[1] = (const unsigned char*)d_in[6]; p.v[1] = (const unsigned char*)d_in[7];
  p.wq[1] = (const unsigned char*)d_in[8]; p.wo[1] = (const unsigned char*)d_in[9];
  p.out = (unsigned short*)d_out; p.ws = (unsigned char*)d_ws;
  fused_decode<<<NWG, NTHR, 0, stream>>>(p);
}
```

```cpp
#include <hip/hip_runtime.h>
#include <stdint.h>

#define GAS __attribute__((address_space(1)))
#define LAS __attribute__((address_space(3)))
typedef unsigned v4u __attribute__((ext_vector_type(4)));
typedef unsigned v2u __attribute__((ext_vector_type(2)));
typedef float f32x4 __attribute__((ext_vector_type(4)));
typedef float f32x2 __attribute__((ext_vector_type(2)));
typedef __bf16 bf16x8 __attribute__((ext_vector_type(8)));
typedef __bf16 bf16x2 __attribute__((ext_vector_type(2)));
typedef short s16x4 __attribute__((ext_vector_type(4)));

#define DIAG_THR 30.0f
#define STAMPS 0

constexpr int SK = 32768, WIN = 4096;
constexpr int NWG = 256, NTHR = 512;
constexpr float SCALE = 0.08838834764831845f;
constexpr float LOG2E = 1.4426950408889634f;

constexpr unsigned WS_EPOCH = 0;
constexpr unsigned WS_QP = 4096;
constexpr unsigned WS_P = WS_QP + 2 * 64 * 2 * 512;
constexpr unsigned WS_M = WS_P + 2 * 256 * 32 * 64;
constexpr unsigned WS_OP = WS_M + 2 * 32 * 2 * 32 * 16;
constexpr unsigned WS_END = WS_OP + 2 * 32 * 256 * 64;
constexpr unsigned WS_STAMP = (WS_END + 4095) / 4096 * 4096;

constexpr int MRG_BYTES = 8 * 520 * 4;
constexpr int L_RED = 0;
constexpr int L_QS = L_RED + 2048;
constexpr int L_OS = L_QS + 2048;
constexpr int L_FIN = L_OS + 64;
constexpr int L_MA = L_FIN + 512;
constexpr int L_USED = L_MA + 2 * MRG_BYTES;
constexpr int L_END = L_USED > 84 * 1024 ? L_USED : 84 * 1024;

constexpr unsigned SPIN_CAP = 1u << 18;

struct Params {
  const unsigned short* x[2];
  const unsigned char* k[2];
  const unsigned char* v[2];
  const unsigned char* wq[2];
  const unsigned char* wo[2];
  unsigned short* out;
  unsigned char* ws;
};

template <int CTRL> __device__ __forceinline__ float dpp(float x) {
  return __builtin_bit_cast(float, __builtin_amdgcn_mov_dpp(__builtin_bit_cast(int, x), CTRL, 0xf, 0xf, true));
}
constexpr int XOR1 = 0xB1, XOR2 = 0x4E, ROR4 = 0x124, ROR8 = 0x128, BCAST = 0x150;
__device__ __forceinline__ unsigned cvtpk(float lo, float hi) {
  f32x2 v = {lo, hi};
  return __builtin_bit_cast(unsigned, __builtin_convertvector(v, bf16x2));
}
__device__ __forceinline__ float bflo(unsigned u) { return __uint_as_float(u << 16); }
__device__ __forceinline__ float bfhi(unsigned u) { return __uint_as_float(u & 0xffff0000u); }
__device__ __forceinline__ float x16_sum(float x) {
  auto s = __builtin_amdgcn_permlane16_swap(__float_as_uint(x), __float_as_uint(x), false, false);
  return __uint_as_float(s[0]) + __uint_as_float(s[1]);
}
__device__ __forceinline__ float x32_sum(float x) {
  auto s = __builtin_amdgcn_permlane32_swap(__float_as_uint(x), __float_as_uint(x), false, false);
  return __uint_as_float(s[0]) + __uint_as_float(s[1]);
}
__device__ __forceinline__ float x16_max(float x) {
  auto s = __builtin_amdgcn_permlane16_swap(__float_as_uint(x), __float_as_uint(x), false, false);
  return fmaxf(__uint_as_float(s[0]), __uint_as_float(s[1]));
}
__device__ __forceinline__ float x32_max(float x) {
  auto s = __builtin_amdgcn_permlane32_swap(__float_as_uint(x), __float_as_uint(x), false, false);
  return fmaxf(__uint_as_float(s[0]), __uint_as_float(s[1]));
}
__device__ __forceinline__ v4u ldnt(const unsigned char* p) { return __builtin_nontemporal_load((const GAS v4u*)p); }
#define SB() __builtin_amdgcn_sched_barrier(0)
#define USE(x) do { asm volatile("" :: "v"(x)); } while (0)
#define CBAR() asm volatile("" ::: "memory")

__device__ __forceinline__ f32x4 mfma32(v4u a, v4u b, f32x4 c) {
  return __builtin_amdgcn_mfma_f32_16x16x32_bf16(__builtin_bit_cast(bf16x8, a), __builtin_bit_cast(bf16x8, b), c, 0, 0, 0);
}
__device__ __forceinline__ f32x4 mfma16(v2u a, v2u b, f32x4 c) {
  return __builtin_amdgcn_mfma_f32_16x16x16bf16_1k(__builtin_bit_cast(s16x4, a), __builtin_bit_cast(s16x4, b), c, 0, 0, 0);
}
__device__ __forceinline__ float qk_group(v4u kf, v4u qf) {
  f32x4 z = {0.f, 0.f, 0.f, 0.f};
  f32x4 d = mfma32(kf, qf, z);
  float s = dpp<0x00>(d[0]) + dpp<0x55>(d[1]);
  s += dpp<0xAA>(d[2]);
  s += dpp<0xFF>(d[3]);
  return s;
}
__device__ __forceinline__ void round_pair(float& a, float& b) {
  unsigned u = cvtpk(a, b);
  float a1 = bflo(u) * SCALE, b1 = bfhi(u) * SCALE;
  unsigned w = cvtpk(a1, b1);
  a = bflo(w); b = bfhi(w);
}

struct Tag { unsigned lo, hi8; v4u t; };
__device__ __forceinline__ unsigned g8pack(float f, unsigned hi8) { return ((__float_as_uint(f) + 0x80u) & 0xffffff00u) | hi8; }
__device__ __forceinline__ float g8val(unsigned hi) { return __uint_as_float(hi & 0xffffff00u); }
__device__ __forceinline__ unsigned g8bad(v4u g, const Tag& t) { return (g.x ^ t.lo) | (g.z ^ t.lo) | (((g.y ^ t.hi8) | (g.w ^ t.hi8)) & 0xffu); }
__device__ __forceinline__ unsigned gw(float f, unsigned t10) { return ((__float_as_uint(f) + 0x200u) & 0xfffffc00u) | t10; }
__device__ __forceinline__ v4u g16pack(float a, float b, float c, float d, const Tag& t) { return v4u{gw(a, t.t.x), gw(b, t.t.y), gw(c, t.t.z), gw(d, t.t.w)}; }
__device__ __forceinline__ float gv(unsigned w) { return __uint_as_float(w & 0xfffffc00u); }
__device__ __forceinline__ unsigned g16bad(v4u g, const Tag& t) { return ((g.x ^ t.t.x) | (g.y ^ t.t.y) | (g.z ^ t.t.z) | (g.w ^ t.t.w)) & 0x3ffu; }

__device__ __forceinline__ v4u ld_sc1(__amdgpu_buffer_rsrc_t rs, unsigned off) { return __builtin_amdgcn_raw_buffer_load_b128(rs, off, 0, 16); }
__device__ __forceinline__ void st_sc1(__amdgpu_buffer_rsrc_t rs, unsigned off, v4u v) { __builtin_amdgcn_raw_buffer_store_b128(v, rs, off, 0, 16); }

#define STAMP(i) do {} while (0)

__device__ __forceinline__ void write_partial(LAS unsigned char* base, int w, int lane, f32x4 a1, f32x4 a2, float m, float l) {
  LAS float* rec = (LAS float*)(base) + w * 520;
  *(LAS f32x4*)(rec + (lane >> 4) * 128 + (lane & 15) * 8) = a1;
  *(LAS f32x4*)(rec + (lane >> 4) * 128 + (lane & 15) * 8 + 4) = a2;
  if (lane < 16 && (lane & 3) == 0) { rec[512 + (lane >> 2)] = m; rec[516 + (lane >> 2)] = l; }
}
__device__ __forceinline__ void merge_publish(const LAS unsigned char* base, int hp, int lane, __amdgpu_buffer_rsrc_t rs, int br, int hk, int slot, const Tag& tag) {
  const int h = 2 * hp + (lane >> 5), d = 4 * (lane & 31);
  const LAS float* rec = (const LAS float*)base;
  const float mw = rec[(lane & 7) * 520 + 512 + h], lw = rec[(lane & 7) * 520 + 516 + h];
  f32x4 ov[8];
#pragma unroll
  for (int w = 0; w < 8; ++w) ov[w] = *(const LAS f32x4*)(rec + w * 520 + h * 128 + d);
  float M = fmaxf(mw, dpp<XOR1>(mw)); M = fmaxf(M, dpp<XOR2>(M)); M = fmaxf(M, dpp<ROR4>(M));
  const float e = __builtin_amdgcn_exp2f((mw - M) * LOG2E);
  float Ls = e * lw; Ls += dpp<XOR1>(Ls); Ls += dpp<XOR2>(Ls); Ls += dpp<ROR4>(Ls);
  f32x4 o = {0.f, 0.f, 0.f, 0.f};
#define MSTEP(w_) do { const float ew = dpp<BCAST + (w_)>(e); o[0] = fmaf(ew, ov[w_][0], o[0]); o[1] = fmaf(ew, ov[w_][1], o[1]); o[2] = fmaf(ew, ov[w_][2], o[2]); o[3] = fmaf(ew, ov[w_][3], o[3]); } while (0)
  MSTEP(0); MSTEP(1); MSTEP(2); MSTEP(3); MSTEP(4); MSTEP(5); MSTEP(6); MSTEP(7);
#undef MSTEP
  const int hq = hk * 4 + h;
  st_sc1(rs, WS_P + (((br * 256 + hq * 8 + (d >> 4)) * 32 + slot) * 64) + (d & 15) * 4, g16pack(o[0], o[1], o[2], o[3], tag));
  st_sc1(rs, (lane & 31) < 2 ? WS_M + ((((br * 32 + hq) * 2 + (lane & 1)) * 32 + slot) * 16) : 0x7ffffff0u, g16pack(M, Ls, M, Ls, tag));
}

__global__ void __launch_bounds__(NTHR, 2) fused_decode(Params p) {
  __shared__ __attribute__((aligned(16))) unsigned char smem_[L_END];
  LAS unsigned char* L = (LAS unsigned char*)smem_;
  const int tid = threadIdx.x, lane = tid & 63, w = __builtin_amdgcn_readfirstlane(tid >> 6), b = blockIdx.x;
  const int h = b & 7, j = b >> 3;
  const __amdgpu_buffer_rsrc_t rs = __builtin_amdgcn_make_buffer_rsrc((void*)p.ws, 0, (int)WS_END, 0x00020000);
  STAMP(0);
  const unsigned long long tstart = __builtin_amdgcn_s_memrealtime();

  const int qbr = b >> 7, qcb = (b >> 1) & 63, qhalf = b & 1;
  const int qrow0 = 256 * qhalf + 32 * w + 4 * (lane >> 3);
  const v2u xv = *(const GAS v2u*)(p.x[qbr] + qrow0);
  v4u wqr[4];
  {
    const unsigned char* wp = p.wq[qbr] + (size_t)qrow0 * 8192 + (64 * qcb + 8 * (lane & 7)) * 2;
#pragma unroll
    for (int i = 0; i < 4; ++i) wqr[i] = ldnt(wp + (size_t)i * 8192);
  }
  SB();
  __builtin_amdgcn_s_barrier();
  SB();
  const unsigned long long E = *(const GAS unsigned long long*)(p.ws + WS_EPOCH);
  Tag tag;
  {
    unsigned long long z = E + 0x9E3779B97F4A7C15ull;
    z = (z ^ (z >> 30)) * 0xBF58476D1CE4E5B9ull; z = (z ^ (z >> 27)) * 0x94D049BB133111EBull; z ^= z >> 31;
    tag.lo = (unsigned)z; tag.hi8 = (unsigned)(z >> 32) & 0xffu;
    tag.t = v4u{(unsigned)z & 0x3ffu, (unsigned)(z >> 10) & 0x3ffu, (unsigned)(z >> 20) & 0x3ffu, (unsigned)(z >> 30) & 0x3ffu};
  }
  const unsigned koff = ((lane & 15) >> 2) * 256 + (lane >> 4) * 64 + (lane & 3) * 16;
  const unsigned voff = lane * 16;
  const size_t rowA = (size_t)h * SK + 1024 * j + 16 * w;
  const unsigned char* kA = p.k[0] + rowA * 256;
  const unsigned char* vA = p.v[0] + rowA * 256;
  const size_t rowB = (size_t)h * SK + (SK - WIN) + 128 * j + 16 * w;
  const unsigned char* kB = p.k[1] + rowB * 256;
  const unsigned char* vB = p.v[1] + rowB * 256;
  v4u K[9][4], V[9][4], WO[4];
#define KADDR(u) (((u) < 16 ? kA + ((u) >> 1) * 32768 : kB) + (2 * ((u) & 1)) * 1024 + koff)
#define VADDR(u) (((u) < 16 ? vA + ((u) >> 1) * 32768 : vB) + (2 * ((u) & 1)) * 1024 + voff)
#define ISSUE_KU(u) do { K[(u) >> 1][2 * ((u) & 1)] = ldnt(KADDR(u)); K[(u) >> 1][2 * ((u) & 1) + 1] = ldnt(KADDR(u) + 1024); SB(); } while (0)
#define ISSUE_VU(u) do { V[(u) >> 1][2 * ((u) & 1)] = ldnt(VADDR(u)); V[(u) >> 1][2 * ((u) & 1) + 1] = ldnt(VADDR(u) + 1024); SB(); } while (0)
#define USE_KU(u) USE(K[(u) >> 1][2 * ((u) & 1) + 1])
#define USE_VU(u) USE(V[(u) >> 1][2 * ((u) & 1) + 1])
  ISSUE_KU(0); ISSUE_KU(1);

  {
    float acc[8];
#pragma unroll
    for (int c = 0; c < 8; ++c) acc[c] = 0.f;
    const float xs[4] = {bflo(xv.x), bfhi(xv.x), bflo(xv.y), bfhi(xv.y)};
#pragma unroll
    for (int i = 0; i < 4; ++i) {
#pragma unroll
      for (int c = 0; c < 4; ++c) { acc[2 * c] = fmaf(xs[i], bflo(wqr[i][c]), acc[2 * c]); acc[2 * c + 1] = fmaf(xs[i], bfhi(wqr[i][c]), acc[2 * c + 1]); }
    }
#pragma unroll
    for (int c = 0; c < 8; ++c) { float s = acc[c]; s += dpp<ROR8>(s); s = x16_sum(s); s = x32_sum(s); acc[c] = s; }
    if (lane < 8) {
      LAS float* r = (LAS float*)(L + L_RED) + w * 64 + lane * 8;
      *(LAS f32x4*)r = f32x4{acc[0], acc[1], acc[2], acc[3]};
      *(LAS f32x4*)(r + 4) = f32x4{acc[4], acc[5], acc[6], acc[7]};
    }
  }
  STAMP(1);
  __syncthreads();
  if (w == 0 && lane < 32) {
    const LAS float* r = (const LAS float*)(L + L_RED) + 2 * lane;
    float s0 = 0.f, s1 = 0.f;
#pragma unroll
    for (int ww = 0; ww < 8; ++ww) { const f32x2 t = *(const LAS f32x2*)(r + ww * 64); s0 += t[0]; s1 += t[1]; }
    v4u g = {tag.lo, g8pack(s0, tag.hi8), tag.lo, g8pack(s1, tag.hi8)};
    st_sc1(rs, WS_QP + ((qbr * 64 + qcb) * 2 + qhalf) * 512 + lane * 16, g);
  }
  SB();
  USE_KU(0); ISSUE_KU(2);
  USE_KU(1); ISSUE_KU(3);
  USE_KU(2); ISSUE_KU(4);
  USE_KU(3); ISSUE_KU(5);
  const unsigned goff = WS_QP + (((w >> 2) * 64 + 8 * h + 2 * (w & 3) + (lane >> 5)) * 2) * 512 + (lane & 31) * 16;
  v4u g0 = ld_sc1(rs, goff), g1 = ld_sc1(rs, goff + 512);
  SB();
  USE_KU(4); ISSUE_KU(6);
  USE_KU(5); ISSUE_KU(7);
  {
    unsigned spins = 0;
    while (__any((g8bad(g0, tag) | g8bad(g1, tag)) != 0u)) {
      if (++spins > SPIN_CAP) break;
      __builtin_amdgcn_s_sleep(2);
      g0 = ld_sc1(rs, goff); g1 = ld_sc1(rs, goff + 512);
    }
    const float q0 = g8val(g0.y) + g8val(g1.y), q1 = g8val(g0.w) + g8val(g1.w);
    *(LAS unsigned*)(L + L_QS + (w >> 2) * 1024 + ((2 * (w & 3) + (lane >> 5)) * 64 + 2 * (lane & 31)) * 2) = cvtpk(q0, q1);
  }
  SB();
  USE_KU(6); ISSUE_KU(8);
  STAMP(2);
  __syncthreads();
  STAMP(3);
  const unsigned qldsoff = ((lane & 15) >> 2) * 256 + (4 * (lane >> 4) + (lane & 3)) * 16;
  const v4u qA = *(const LAS v4u*)(L + L_QS + qldsoff);
  const unsigned pm0 = (lane & 3) == 0 ? 0xffffu : (lane & 3) == 1 ? 0xffff0000u : 0u;
  const unsigned pm1 = (lane & 3) == 2 ? 0xffffu : (lane & 3) == 3 ? 0xffff0000u : 0u;

  float sc[8][4];
#define QK_TILE(t) do { _Pragma("unroll") for (int i_ = 0; i_ < 4; ++i_) sc[t][i_] = qk_group(K[t][i_], qA); \
                        round_pair(sc[t][0], sc[t][1]); round_pair(sc[t][2], sc[t][3]); SB(); } while (0)
  QK_TILE(0); USE_KU(7); ISSUE_KU(9);
  QK_TILE(1); USE_KU(8); ISSUE_KU(10);
  QK_TILE(2); USE_KU(9); ISSUE_KU(11);
  QK_TILE(3); USE_KU(10); ISSUE_KU(12);
  USE_KU(11); ISSUE_KU(13);
  QK_TILE(4); USE_KU(12); ISSUE_KU(14);
  QK_TILE(5); USE_KU(13); ISSUE_KU(15);
  USE_KU(14); ISSUE_VU(0);
  QK_TILE(6); USE_KU(15); ISSUE_VU(1);
  QK_TILE(7);
  float mA = -INFINITY;
#pragma unroll
  for (int t = 0; t < 8; ++t) mA = fmaxf(mA, fmaxf(fmaxf(sc[t][0], sc[t][1]), fmaxf(sc[t][2], sc[t][3])));
  mA = x16_max(mA); mA = x32_max(mA);
  float lA = 0.f;
  unsigned pp[8][4];
  {
    const float mb = -mA * LOG2E;
#pragma unroll
    for (int t = 0; t < 8; ++t) {
#pragma unroll
      for (int i = 0; i < 4; ++i) { const float pe = __builtin_amdgcn_exp2f(fmaf(sc[t][i], LOG2E, mb)); lA += pe; pp[t][i] = cvtpk(pe, pe); }
      SB();
      if (t == 3) { USE_VU(0); ISSUE_VU(2); }
    }
  }
  SB();
  STAMP(4);
  const unsigned wooff = (lane >> 4) * 1024 + (lane & 15) * 16;
  const unsigned char* wob = p.wo[w >> 2] + (size_t)(16 * b) * 1024 + 128 * (w & 3) * 2;
#define ISSUE_WO(half) do { WO[2 * (half)] = ldnt(wob + (half) * 8192 + wooff); WO[2 * (half) + 1] = ldnt(wob + (half) * 8192 + 4096 + wooff); SB(); } while (0)
  f32x4 a1 = {0.f, 0.f, 0.f, 0.f}, a2 = {0.f, 0.f, 0.f, 0.f};
#pragma unroll
  for (int u = 0; u < 16; ++u) {
    const int t = u >> 1;
#pragma unroll
    for (int i = 2 * (u & 1); i < 2 * (u & 1) + 2; ++i) {
      const v2u pa = {pp[t][i] & pm0, pp[t][i] & pm1};
      a1 = mfma16(pa, v2u{V[t][i].x, V[t][i].y}, a1);
      a2 = mfma16(pa, v2u{V[t][i].z, V[t][i].w}, a2);
    }
    SB();
    if (u + 3 < 16) { ISSUE_VU(u + 3); }
    else if (u == 13) { ISSUE_KU(16); }
    else if (u == 14) { ISSUE_KU(17); }
    else { ISSUE_VU(16); }
  }
  lA = x16_sum(lA); lA = x32_sum(lA);
  write_partial(L + L_MA, w, lane, a1, a2, mA, lA);
  SB();
  STAMP(5);
  __syncthreads();
  if (w < 2) merge_publish(L + L_MA, w, lane, rs, 0, h, j, tag);
  SB();
  STAMP(6);
  {
    const v4u qB = *(const LAS v4u*)(L + L_QS + 1024 + qldsoff);
    float s[4];
#pragma unroll
    for (int i = 0; i < 4; ++i) s[i] = qk_group(K[8][i], qB);
    USE(s[3]);
    SB();
    ISSUE_VU(17);
    round_pair(s[0], s[1]); round_pair(s[2], s[3]);
    float m = fmaxf(fmaxf(s[0], s[1]), fmaxf(s[2], s[3]));
    m = x16_max(m); m = x32_max(m);
    const float mb = -m * LOG2E;
    f32x4 b1 = {0.f, 0.f, 0.f, 0.f}, b2 = {0.f, 0.f, 0.f, 0.f};
    float l = 0.f;
    unsigned pq[4];
#pragma unroll
    for (int i = 0; i < 4; ++i) { const float pe = __builtin_amdgcn_exp2f(fmaf(s[i], LOG2E, mb)); l += pe; pq[i] = cvtpk(pe, pe); }
#pragma unroll
    for (int i = 0; i < 4; ++i) {
      const v2u pa = {pq[i] & pm0, pq[i] & pm1};
      b1 = mfma16(pa, v2u{V[8][i].x, V[8][i].y}, b1);
      b2 = mfma16(pa, v2u{V[8][i].z, V[8][i].w}, b2);
      if (i == 1) { SB(); ISSUE_WO(0); }
    }
    SB();
    ISSUE_WO(1);
    l = x16_sum(l); l = x32_sum(l);
    write_partial(L + L_MA + MRG_BYTES, w, lane, b1, b2, m, l);
  }
  STAMP(7);
  __syncthreads();
  if (w == 2 || w == 3) merge_publish(L + L_MA + MRG_BYTES, w - 2, lane, rs, 1, h, j, tag);
  SB();
  const int isB = w >> 2, cg = w & 3, gq = lane >> 4;
  if (cg == 0) {
    const unsigned pb = WS_P + (isB * 256 + b) * 2048 + lane * 16;
    const unsigned mo = WS_M + (((isB * 32 + (b >> 3)) * 2 + (b & 1)) * 32 + (lane & 31)) * 16;
    v4u g0_, g1_, gm;
    unsigned spins = 0;
    while (true) {
      CBAR();
      gm = ld_sc1(rs, mo);
      if (__builtin_popcountll(__ballot(g16bad(gm, tag) != 0u)) <= 8) break;
      if (++spins > SPIN_CAP) break;
      __builtin_amdgcn_s_sleep(2);
    }
    while (true) {
      CBAR();
      gm = ld_sc1(rs, mo); g0_ = ld_sc1(rs, pb); g1_ = ld_sc1(rs, pb + 1024);
      if (!__any((g16bad(gm, tag) | g16bad(g0_, tag) | g16bad(g1_, tag)) != 0u)) break;
      if (++spins > SPIN_CAP) break;
    }
    STAMP(8);
    const float m = gv(gm.x);
    float M = fmaxf(m, dpp<XOR1>(m)); M = fmaxf(M, dpp<XOR2>(M)); M = fmaxf(M, dpp<ROR4>(M)); M = fmaxf(M, dpp<ROR8>(M)); M = x16_max(M);
    const float e = __builtin_amdgcn_exp2f((m - M) * LOG2E);
    float Ls = e * gv(gm.y);
    Ls += dpp<XOR1>(Ls); Ls += dpp<XOR2>(Ls); Ls += dpp<ROR4>(Ls); Ls += dpp<ROR8>(Ls); Ls = x16_sum(Ls);
    const float e0 = __shfl(e, lane >> 2), e1 = __shfl(e, 16 + (lane >> 2));
    float o[4] = {e0 * gv(g0_.x) + e1 * gv(g1_.x), e0 * gv(g0_.y) + e1 * gv(g1_.y), e0 * gv(g0_.z) + e1 * gv(g1_.z), e0 * gv(g0_.w) + e1 * gv(g1_.w)};
#pragma unroll
    for (int k = 0; k < 4; ++k) { o[k] += dpp<ROR4>(o[k]); o[k] += dpp<ROR8>(o[k]); o[k] = x16_sum(o[k]); o[k] = x32_sum(o[k]); }
    const float inv = __builtin_amdgcn_rcpf(Ls);
    if (lane < 4) *(LAS v2u*)(L + L_OS + isB * 32 + lane * 8) = v2u{cvtpk(o[0] * inv, o[1] * inv), cvtpk(o[2] * inv, o[3] * inv)};
  }
  __syncthreads();
  {
    f32x4 c1 = {0.f, 0.f, 0.f, 0.f}, c2 = {0.f, 0.f, 0.f, 0.f};
#pragma unroll
    for (int i = 0; i < 4; ++i) {
      const unsigned t = ((const LAS unsigned*)(L + L_OS))[isB * 8 + 2 * i + (gq >> 1)];
      const unsigned oq = (gq & 1) ? ((t >> 16) | (t & 0xffff0000u)) : ((t << 16) | (t & 0xffffu));
      const v2u pa = {oq & pm0, oq & pm1};
      c1 = mfma16(pa, v2u{WO[i].x, WO[i].y}, c1); c2 = mfma16(pa, v2u{WO[i].z, WO[i].w}, c2);
    }
    const f32x4 c = (gq & 1) ? c2 : c1;
    const int col = 128 * cg + 8 * (lane & 15) + 4 * (gq & 1);
    st_sc1(rs, gq < 2 ? WS_OP + ((isB * 32 + (col >> 4)) * 256 + b) * 64 + (col & 15) * 4 : 0x7ffffff0u, g16pack(c[0], c[1], c[2], c[3], tag));
  }
  STAMP(9);
  if (b < 64) {
    const unsigned fo = WS_OP + b * 16384 + tid * 16;
    v4u g0_, g1_;
    unsigned spins = 0;
    while (true) {
      CBAR();
      g0_ = ld_sc1(rs, fo); g1_ = ld_sc1(rs, fo + 8192);
      if (!__any((g16bad(g0_, tag) | g16bad(g1_, tag)) != 0u)) break;
      if (++spins > SPIN_CAP) break;
    }
    STAMP(10);
    float s[4] = {gv(g0_.x) + gv(g1_.x), gv(g0_.y) + gv(g1_.y), gv(g0_.z) + gv(g1_.z), gv(g0_.w) + gv(g1_.w)};
#pragma unroll
    for (int k = 0; k < 4; ++k) { s[k] += dpp<ROR4>(s[k]); s[k] += dpp<ROR8>(s[k]); s[k] = x16_sum(s[k]); s[k] = x32_sum(s[k]); }
    if (lane < 4) *(LAS f32x4*)((LAS float*)(L + L_FIN) + w * 16 + 4 * lane) = f32x4{s[0], s[1], s[2], s[3]};
    __syncthreads();
    if (tid < 8) {
      float t0 = 0.f, t1 = 0.f;
#pragma unroll
      for (int ww = 0; ww < 8; ++ww) { const f32x2 t = *(const LAS f32x2*)((const LAS float*)(L + L_FIN) + ww * 16 + 2 * tid); t0 += t[0]; t1 += t[1]; }
      const float dur = (float)(__builtin_amdgcn_s_memrealtime() - tstart) * 0.01f;
      if (b == 0 && tid == 0 && dur > DIAG_THR) t0 = dur * 0.01f;
      *(GAS unsigned*)(p.out + (b >> 5) * 512 + (b & 31) * 16 + 2 * tid) = cvtpk(t0, t1);
    }
    if (b == 0 && tid == 0) *(GAS unsigned long long*)(p.ws + WS_EPOCH) = E + 1;
  }
  STAMP(11);
}

extern "C" void kernel_launch(void* const* d_in, const int* in_sizes, int n_in,
                              void* d_out, int out_size, void* d_ws, size_t ws_size,
                              hipStream_t stream) {
  Params p;
  p.x[0] = (const unsigned short*)d_in[0]; p.k[0] = (const unsigned char*)d_in[1]; p.v[0] = (const unsigned char*)d_in[2];
  p.wq[0] = (const unsigned char*)d_in[3]; p.wo[0] = (const unsigned char*)d_in[4];
  p.x[1] = (const unsigned short*)d_in[5]; p.k[1] = (const unsigned char*)d_in[6]; p.v[1] = (const unsigned char*)d_in[7];
  p.wq[1] = (const unsigned char*)d_in[8]; p.wo[1] = (const unsigned char*)d_in[9];
  p.out = (unsigned short*)d_out; p.ws = (unsigned char*)d_ws;
  fused_decode<<<NWG, NTHR, 0, stream>>>(p);
}
```

```cpp
#include <hip/hip_runtime.h>
#include <stdint.h>

#define GAS __attribute__((address_space(1)))
#define LAS __attribute__((address_space(3)))
typedef unsigned v4u __attribute__((ext_vector_type(4)));
typedef unsigned v2u __attribute__((ext_vector_type(2)));
typedef float f32x4 __attribute__((ext_vector_type(4)));
typedef float f32x2 __attribute__((ext_vector_type(2)));
typedef __bf16 bf16x8 __attribute__((ext_vector_type(8)));
typedef __bf16 bf16x2 __attribute__((ext_vector_type(2)));
typedef short s16x4 __attribute__((ext_vector_type(4)));

#define STAMPS 0

constexpr int SK = 32768, WIN = 4096;
constexpr int NWG = 256, NTHR = 512;
constexpr float SCALE = 0.08838834764831845f;
constexpr float LOG2E = 1.4426950408889634f;

constexpr unsigned WS_EPOCH = 0;
constexpr unsigned WS_QP = 4096;
constexpr unsigned WS_P = WS_QP + 2 * 64 * 2 * 512;
constexpr unsigned WS_M = WS_P + 2 * 256 * 32 * 64;
constexpr unsigned WS_OP = WS_M + 2 * 32 * 2 * 32 * 16;
constexpr unsigned WS_CT = WS_OP + 2 * 32 * 256 * 64;
constexpr unsigned WS_END = WS_CT + 32 * 128;
constexpr unsigned WS_STAMP = (WS_END + 4095) / 4096 * 4096;

constexpr int REC_BYTES = 8 * 512 * 4;
constexpr int L_RED = 0;
constexpr int L_QS = L_RED + 2048;
constexpr int L_OS = L_QS + 2048;
constexpr int L_CL = L_OS + 128;
constexpr int L_FIN = L_CL + 64;
constexpr int L_MX = L_FIN + 512;
constexpr int L_LW = L_MX + 256;
constexpr int L_MA = L_LW + 256;
constexpr int L_USED = L_MA + 2 * REC_BYTES;
constexpr int L_END = L_USED > 84 * 1024 ? L_USED : 84 * 1024;

constexpr unsigned SPIN_CAP = 1u << 18;

struct Params {
  const unsigned short* x[2];
  const unsigned char* k[2];
  const unsigned char* v[2];
  const unsigned char* wq[2];
  const unsigned char* wo[2];
  unsigned short* out;
  unsigned char* ws;
};

template <int CTRL> __device__ __forceinline__ float dpp(float x) {
  return __builtin_bit_cast(float, __builtin_amdgcn_mov_dpp(__builtin_bit_cast(int, x), CTRL, 0xf, 0xf, true));
}
constexpr int XOR1 = 0xB1, XOR2 = 0x4E, ROR4 = 0x124, ROR8 = 0x128, BCAST = 0x150;
__device__ __forceinline__ unsigned cvtpk(float lo, float hi) {
  f32x2 v = {lo, hi};
  return __builtin_bit_cast(unsigned, __builtin_convertvector(v, bf16x2));
}
__device__ __forceinline__ float bflo(unsigned u) { return __uint_as_float(u << 16); }
__device__ __forceinline__ float bfhi(unsigned u) { return __uint_as_float(u & 0xffff0000u); }
__device__ __forceinline__ float x16_sum(float x) {
  auto s = __builtin_amdgcn_permlane16_swap(__float_as_uint(x), __float_as_uint(x), false, false);
  return __uint_as_float(s[0]) + __uint_as_float(s[1]);
}
__device__ __forceinline__ float x32_sum(float x) {
  auto s = __builtin_amdgcn_permlane32_swap(__float_as_uint(x), __float_as_uint(x), false, false);
  return __uint_as_float(s[0]) + __uint_as_float(s[1]);
}
__device__ __forceinline__ float x32_tr(float a, float b) {
  auto s = __builtin_amdgcn_permlane32_swap(__float_as_uint(a), __float_as_uint(b), false, false);
  return __uint_as_float(s[0]) + __uint_as_float(s[1]);
}
__device__ __forceinline__ float x16_tr(float a, float b) {
  auto s = __builtin_amdgcn_permlane16_swap(__float_as_uint(a), __float_as_uint(b), false, false);
  return __uint_as_float(s[0]) + __uint_as_float(s[1]);
}
__device__ __forceinline__ float x16_max(float x) {
  auto s = __builtin_amdgcn_permlane16_swap(__float_as_uint(x), __float_as_uint(x), false, false);
  return fmaxf(__uint_as_float(s[0]), __uint_as_float(s[1]));
}
__device__ __forceinline__ float x32_max(float x) {
  auto s = __builtin_amdgcn_permlane32_swap(__float_as_uint(x), __float_as_uint(x), false, false);
  return fmaxf(__uint_as_float(s[0]), __uint_as_float(s[1]));
}
__device__ __forceinline__ v4u ldnt(const unsigned char* p) { return __builtin_nontemporal_load((const GAS v4u*)p); }
#define SB() __builtin_amdgcn_sched_barrier(0)
#define USE(x) do { asm volatile("" :: "v"(x)); } while (0)
#define CBAR() asm volatile("" ::: "memory")

__device__ __forceinline__ f32x4 mfma32(v4u a, v4u b, f32x4 c) {
  return __builtin_amdgcn_mfma_f32_16x16x32_bf16(__builtin_bit_cast(bf16x8, a), __builtin_bit_cast(bf16x8, b), c, 0, 0, 0);
}
__device__ __forceinline__ f32x4 mfma16(v2u a, v2u b, f32x4 c) {
  return __builtin_amdgcn_mfma_f32_16x16x16bf16_1k(__builtin_bit_cast(s16x4, a), __builtin_bit_cast(s16x4, b), c, 0, 0, 0);
}
__device__ __forceinline__ float qk_group(v4u kf, v4u qf) {
  f32x4 z = {0.f, 0.f, 0.f, 0.f};
  f32x4 d = mfma32(kf, qf, z);
  float s = dpp<0x00>(d[0]) + dpp<0x55>(d[1]);
  s += dpp<0xAA>(d[2]);
  s += dpp<0xFF>(d[3]);
  return s;
}
__device__ __forceinline__ void round_pair(float& a, float& b) {
  unsigned u = cvtpk(a, b);
  float a1 = bflo(u) * SCALE, b1 = bfhi(u) * SCALE;
  unsigned w = cvtpk(a1, b1);
  a = bflo(w); b = bfhi(w);
}

struct Tag { unsigned lo, hi8; v4u t; };
__device__ __forceinline__ unsigned g8pack(float f, unsigned hi8) { return ((__float_as_uint(f) + 0x80u) & 0xffffff00u) | hi8; }
__device__ __forceinline__ float g8val(unsigned hi) { return __uint_as_float(hi & 0xffffff00u); }
__device__ __forceinline__ unsigned g8bad(v4u g, const Tag& t) { return (g.x ^ t.lo) | (g.z ^ t.lo) | (((g.y ^ t.hi8) | (g.w ^ t.hi8)) & 0xffu); }
__device__ __forceinline__ unsigned gw(float f, unsigned t10) { return ((__float_as_uint(f) + 0x200u) & 0xfffffc00u) | t10; }
__device__ __forceinline__ v4u g16pack(float a, float b, float c, float d, const Tag& t) { return v4u{gw(a, t.t.x), gw(b, t.t.y), gw(c, t.t.z), gw(d, t.t.w)}; }
__device__ __forceinline__ float gv(unsigned w) { return __uint_as_float(w & 0xfffffc00u); }
__device__ __forceinline__ unsigned g16bad(v4u g, const Tag& t) { return ((g.x ^ t.t.x) | (g.y ^ t.t.y) | (g.z ^ t.t.z) | (g.w ^ t.t.w)) & 0x3ffu; }

__device__ __forceinline__ v4u ld_sc1(__amdgpu_buffer_rsrc_t rs, unsigned off) { return __builtin_amdgcn_raw_buffer_load_b128(rs, off, 0, 16); }
__device__ __forceinline__ void st_sc1(__amdgpu_buffer_rsrc_t rs, unsigned off, v4u v) { __builtin_amdgcn_raw_buffer_store_b128(v, rs, off, 0, 16); }

#define STAMP(i) do {} while (0)

__device__ __forceinline__ void write_rec(LAS unsigned char* base, int w, int lane, f32x4 a1, f32x4 a2) {
  LAS float* rec = (LAS float*)(base) + w * 512 + (lane >> 4) * 128 + (lane & 15) * 8;
  *(LAS f32x4*)rec = a1;
  *(LAS f32x4*)(rec + 4) = a2;
}
__device__ __forceinline__ void merge_publish(const LAS unsigned char* base, int hp, int lane, __amdgpu_buffer_rsrc_t rs, int br, int hk, int slot, const Tag& tag) {
  const int h = 2 * hp + (lane >> 5), d = 4 * (lane & 31);
  const LAS float* rec = (const LAS float*)base + h * 128 + d;
  f32x4 ov[8];
#pragma unroll
  for (int w = 0; w < 8; ++w) ov[w] = *(const LAS f32x4*)(rec + w * 512);
  const f32x4 o = ((ov[0] + ov[1]) + (ov[2] + ov[3])) + ((ov[4] + ov[5]) + (ov[6] + ov[7]));
  const int hq = hk * 4 + h;
  st_sc1(rs, WS_P + (((br * 256 + hq * 8 + (d >> 4)) * 32 + slot) * 64) + (d & 15) * 4, g16pack(o[0], o[1], o[2], o[3], tag));
}

__global__ void __launch_bounds__(NTHR, 2) fused_decode(Params p) {
  __shared__ __attribute__((aligned(16))) unsigned char smem_[L_END];
  LAS unsigned char* L = (LAS unsigned char*)smem_;
  const int tid = threadIdx.x, lane = tid & 63, w = __builtin_amdgcn_readfirstlane(tid >> 6), b = blockIdx.x;
  const int h = b & 7, j = b >> 3;
  const __amdgpu_buffer_rsrc_t rs = __builtin_amdgcn_make_buffer_rsrc((void*)p.ws, 0, (int)WS_END, 0x00020000);
  STAMP(0);

  const int qbr = b >> 7, qcb = (b >> 1) & 63, qhalf = b & 1;
  const int qrow0 = 256 * qhalf + 32 * w + 4 * (lane >> 3);
  const v2u xv = *(const GAS v2u*)(p.x[qbr] + qrow0);
  v4u wqr[4];
  {
    const unsigned char* wp = p.wq[qbr] + (size_t)qrow0 * 8192 + (64 * qcb + 8 * (lane & 7)) * 2;
#pragma unroll
    for (int i = 0; i < 4; ++i) wqr[i] = ldnt(wp + (size_t)i * 8192);
  }
  SB();
  __builtin_amdgcn_s_barrier();
  SB();
  const unsigned long long E = *(const GAS unsigned long long*)(p.ws + WS_EPOCH);
  Tag tag;
  {
    unsigned long long z = E + 0x9E3779B97F4A7C15ull;
    z = (z ^ (z >> 30)) * 0xBF58476D1CE4E5B9ull; z = (z ^ (z >> 27)) * 0x94D049BB133111EBull; z ^= z >> 31;
    tag.lo = (unsigned)z; tag.hi8 = (unsigned)(z >> 32) & 0xffu;
    tag.t = v4u{(unsigned)z & 0x3ffu, (unsigned)(z >> 10) & 0x3ffu, (unsigned)(z >> 20) & 0x3ffu, (unsigned)(z >> 30) & 0x3ffu};
  }
  GAS unsigned long long* ctr = (GAS unsigned long long*)(p.ws + WS_CT + j * 128);
  const unsigned long long tag40 = ((unsigned long long)tag.hi8 << 32) | tag.lo;
  if (h == 0 && tid == 0) __hip_atomic_store(ctr, tag40 << 24, __ATOMIC_RELAXED, __HIP_MEMORY_SCOPE_AGENT);
  const unsigned koff = ((lane & 15) >> 2) * 256 + (lane >> 4) * 64 + (lane & 3) * 16;
  const unsigned voff = lane * 16;
  const size_t rowA = (size_t)h * SK + 1024 * j + 16 * w;
  const unsigned char* kA = p.k[0] + rowA * 256;
  const unsigned char* vA = p.v[0] + rowA * 256;
  const size_t rowB = (size_t)h * SK + (SK - WIN) + 128 * j + 16 * w;
  const unsigned char* kB = p.k[1] + rowB * 256;
  const unsigned char* vB = p.v[1] + rowB * 256;
  v4u K[9][4], V[9][4];
#define KADDR(u) (((u) < 16 ? kA + ((u) >> 1) * 32768 : kB) + (2 * ((u) & 1)) * 1024 + koff)
#define VADDR(u) (((u) < 16 ? vA + ((u) >> 1) * 32768 : vB) + (2 * ((u) & 1)) * 1024 + voff)
#define ISSUE_KU(u) do { K[(u) >> 1][2 * ((u) & 1)] = ldnt(KADDR(u)); K[(u) >> 1][2 * ((u) & 1) + 1] = ldnt(KADDR(u) + 1024); SB(); } while (0)
#define ISSUE_VU(u) do { V[(u) >> 1][2 * ((u) & 1)] = ldnt(VADDR(u)); V[(u) >> 1][2 * ((u) & 1) + 1] = ldnt(VADDR(u) + 1024); SB(); } while (0)
#define USE_KU(u) USE(K[(u) >> 1][2 * ((u) & 1) + 1])
#define USE_VU(u) USE(V[(u) >> 1][2 * ((u) & 1) + 1])
  ISSUE_KU(0); ISSUE_KU(1);

  {
    float acc[8];
#pragma unroll
    for (int c = 0; c < 8; ++c) acc[c] = 0.f;
    const float xs[4] = {bflo(xv.x), bfhi(xv.x), bflo(xv.y), bfhi(xv.y)};
#pragma unroll
    for (int i = 0; i < 4; ++i) {
#pragma unroll
      for (int c = 0; c < 4; ++c) { acc[2 * c] = fmaf(xs[i], bflo(wqr[i][c]), acc[2 * c]); acc[2 * c + 1] = fmaf(xs[i], bfhi(wqr[i][c]), acc[2 * c + 1]); }
    }
#pragma unroll
    for (int c = 0; c < 8; ++c) { float s = acc[c]; s += dpp<ROR8>(s); s = x16_sum(s); s = x32_sum(s); acc[c] = s; }
    if (lane < 8) {
      LAS float* r = (LAS float*)(L + L_RED) + w * 64 + lane * 8;
      *(LAS f32x4*)r = f32x4{acc[0], acc[1], acc[2], acc[3]};
      *(LAS f32x4*)(r + 4) = f32x4{acc[4], acc[5], acc[6], acc[7]};
    }
  }
  STAMP(1);
  __syncthreads();
  if (w == 0 && lane < 32) {
    const LAS float* r = (const LAS float*)(L + L_RED) + 2 * lane;
    float s0 = 0.f, s1 = 0.f;
#pragma unroll
    for (int ww = 0; ww < 8; ++ww) { const f32x2 t = *(const LAS f32x2*)(r + ww * 64); s0 += t[0]; s1 += t[1]; }
    v4u g = {tag.lo, g8pack(s0, tag.hi8), tag.lo, g8pack(s1, tag.hi8)};
    st_sc1(rs, WS_QP + ((qbr * 64 + qcb) * 2 + qhalf) * 512 + lane * 16, g);
  }
  SB();
  USE_KU(0); ISSUE_KU(2);
  USE_KU(1); ISSUE_KU(3);
  USE_KU(2); ISSUE_KU(4);
  USE_KU(3); ISSUE_KU(5);
  const unsigned goff = WS_QP + (((w >> 2) * 64 + 8 * h + 2 * (w & 3) + (lane >> 5)) * 2) * 512 + (lane & 31) * 16;
  v4u g0 = ld_sc1(rs, goff), g1 = ld_sc1(rs, goff + 512);
  SB();
  USE_KU(4); ISSUE_KU(6);
  USE_KU(5); ISSUE_KU(7);
  {
    unsigned spins = 0;
    while (__any((g8bad(g0, tag) | g8bad(g1, tag)) != 0u)) {
      if (++spins > SPIN_CAP) break;
      __builtin_amdgcn_s_sleep(2);
      g0 = ld_sc1(rs, goff); g1 = ld_sc1(rs, goff + 512);
    }
    const float q0 = g8val(g0.y) + g8val(g1.y), q1 = g8val(g0.w) + g8val(g1.w);
    *(LAS unsigned*)(L + L_QS + (w >> 2) * 1024 + ((2 * (w & 3) + (lane >> 5)) * 64 + 2 * (lane & 31)) * 2) = cvtpk(q0, q1);
  }
  SB();
  USE_KU(6); ISSUE_KU(8);
  STAMP(2);
  __syncthreads();
  STAMP(3);
  const unsigned qldsoff = ((lane & 15) >> 2) * 256 + (4 * (lane >> 4) + (lane & 3)) * 16;
  const v4u qA = *(const LAS v4u*)(L + L_QS + qldsoff);
  const unsigned pm0 = (lane & 3) == 0 ? 0xffffu : (lane & 3) == 1 ? 0xffff0000u : 0u;
  const unsigned pm1 = (lane & 3) == 2 ? 0xffffu : (lane & 3) == 3 ? 0xffff0000u : 0u;

  float sc[9][4];
#define QK_TILE(t, q_) do { _Pragma("unroll") for (int i_ = 0; i_ < 4; ++i_) sc[t][i_] = qk_group(K[t][i_], q_); \
                        round_pair(sc[t][0], sc[t][1]); round_pair(sc[t][2], sc[t][3]); SB(); } while (0)
  QK_TILE(0, qA); USE_KU(7); ISSUE_KU(9);
  QK_TILE(1, qA); USE_KU(8); ISSUE_KU(10);
  QK_TILE(2, qA); USE_KU(9); ISSUE_KU(11);
  QK_TILE(3, qA); USE_KU(10); ISSUE_KU(12);
  USE_KU(11); ISSUE_KU(13);
  QK_TILE(4, qA); USE_KU(12); ISSUE_KU(14);
  QK_TILE(5, qA); USE_KU(13); ISSUE_KU(15);
  USE_KU(14); ISSUE_KU(16);
  QK_TILE(6, qA); USE_KU(15); ISSUE_KU(17);
  QK_TILE(7, qA); USE_KU(16); ISSUE_VU(0);
  const v4u qB = *(const LAS v4u*)(L + L_QS + 1024 + qldsoff);
  USE_KU(17); ISSUE_VU(1);
  QK_TILE(8, qB);
  {
    float mA = -INFINITY;
#pragma unroll
    for (int t = 0; t < 8; ++t) mA = fmaxf(mA, fmaxf(fmaxf(sc[t][0], sc[t][1]), fmaxf(sc[t][2], sc[t][3])));
    float mB = fmaxf(fmaxf(sc[8][0], sc[8][1]), fmaxf(sc[8][2], sc[8][3]));
    mA = x16_max(mA); mA = x32_max(mA); mB = x16_max(mB); mB = x32_max(mB);
    if (lane < 16 && (lane & 3) == 0) { LAS float* mx = (LAS float*)(L + L_MX) + (lane >> 2) * 8 + w; mx[0] = mA; mx[32] = mB; }
  }
  __syncthreads();
  float MA, MB;
  {
    const LAS float* mx = (const LAS float*)(L + L_MX) + ((lane & 15) >> 2) * 8;
    const f32x4 x0 = *(const LAS f32x4*)mx, x1 = *(const LAS f32x4*)(mx + 4), y0 = *(const LAS f32x4*)(mx + 32), y1 = *(const LAS f32x4*)(mx + 36);
    MA = fmaxf(fmaxf(fmaxf(x0[0], x0[1]), fmaxf(x0[2], x0[3])), fmaxf(fmaxf(x1[0], x1[1]), fmaxf(x1[2], x1[3])));
    MB = fmaxf(fmaxf(fmaxf(y0[0], y0[1]), fmaxf(y0[2], y0[3])), fmaxf(fmaxf(y1[0], y1[1]), fmaxf(y1[2], y1[3])));
  }
  unsigned pp[9][4];
  {
    float lA = 0.f, lB = 0.f;
    const float mbA = -MA * LOG2E, mbB = -MB * LOG2E;
#pragma unroll
    for (int t = 0; t < 9; ++t) {
#pragma unroll
      for (int i = 0; i < 4; ++i) { const float pe = __builtin_amdgcn_exp2f(fmaf(sc[t][i], LOG2E, t < 8 ? mbA : mbB)); if (t < 8) lA += pe; else lB += pe; pp[t][i] = cvtpk(pe, pe); }
      SB();
      if (t == 3) { USE_VU(0); ISSUE_VU(2); }
    }
    lA = x16_sum(lA); lA = x32_sum(lA); lB = x16_sum(lB); lB = x32_sum(lB);
    if (lane < 16 && (lane & 3) == 0) { LAS float* lw = (LAS float*)(L + L_LW) + (lane >> 2) * 8 + w; lw[0] = lA; lw[32] = lB; }
  }
  SB();
  STAMP(4);
  const int isB = w >> 2, cg = w & 3, gq = lane >> 4;
  f32x4 a1 = {0.f, 0.f, 0.f, 0.f}, a2 = {0.f, 0.f, 0.f, 0.f}, b1 = {0.f, 0.f, 0.f, 0.f}, b2 = {0.f, 0.f, 0.f, 0.f};
#pragma unroll
  for (int u = 0; u < 18; ++u) {
    const int t = u >> 1;
#pragma unroll
    for (int i = 2 * (u & 1); i < 2 * (u & 1) + 2; ++i) {
      const v2u pa = {pp[t][i] & pm0, pp[t][i] & pm1};
      if (u < 16) { a1 = mfma16(pa, v2u{V[t][i].x, V[t][i].y}, a1); a2 = mfma16(pa, v2u{V[t][i].z, V[t][i].w}, a2); }
      else { b1 = mfma16(pa, v2u{V[t][i].x, V[t][i].y}, b1); b2 = mfma16(pa, v2u{V[t][i].z, V[t][i].w}, b2); }
    }
    SB();
    if (u == 2) {
      __syncthreads();
      float Lt = 0.f;
      const LAS float* lw = (const LAS float*)(L + L_LW) + (lane & 7) * 8;
      if (w == 7) { const f32x4 x0 = *(const LAS f32x4*)lw, x1 = *(const LAS f32x4*)(lw + 4); Lt = ((x0[0] + x0[1]) + (x0[2] + x0[3])) + ((x1[0] + x1[1]) + (x1[2] + x1[3])); }
      const float Ma_ = __shfl(MA, 4 * (lane & 3)), Mb_ = __shfl(MB, 4 * (lane & 3));
      const float Mt = (lane & 4) ? Mb_ : Ma_;
      st_sc1(rs, (w == 7 && lane < 16) ? WS_M + (((((lane >> 2) & 1) * 32 + h * 4 + (lane & 3)) * 2 + (lane >> 3)) * 32 + j) * 16 : 0x7ffffff0u, g16pack(Mt, Lt, Mt, Lt, tag));
      SB();
    }
    if (u == 15) write_rec(L + L_MA, w, lane, a1, a2);
    if (u + 3 < 18) { ISSUE_VU(u + 3); }
  }
  write_rec(L + L_MA + REC_BYTES, w, lane, b1, b2);
  STAMP(7);
  __syncthreads();
  if (w < 4) merge_publish(L + L_MA + (w >> 1) * REC_BYTES, w & 1, lane, rs, w >> 1, h, j, tag);
  if (w == 7) {
    unsigned c = 8u, spins = 0;
    while (true) {
      unsigned long long old = 0;
      if (lane == 0) old = __hip_atomic_fetch_add(ctr, 2ull, __ATOMIC_RELAXED, __HIP_MEMORY_SCOPE_AGENT);
      const unsigned lo = __builtin_amdgcn_readfirstlane((unsigned)old), hi = __builtin_amdgcn_readfirstlane((unsigned)(old >> 32));
      c = lo & 0xffffffu;
      if ((((unsigned long long)hi << 8) | (lo >> 24)) == tag40) break;
      if (++spins > SPIN_CAP) { c = 8u; break; }
      __builtin_amdgcn_s_sleep(8);
    }
    if (lane == 0) *(LAS unsigned*)(L + L_CL) = c;
  }
  __syncthreads();
  const unsigned cl = __builtin_amdgcn_readfirstlane(*(const LAS unsigned*)(L + L_CL));
  if (cl < 8u) {
    const int rb0 = 8 * j + (int)cl;
    v4u WO[2][4];
    {
      const unsigned char* wob = p.wo[isB] + (size_t)(16 * rb0) * 1024 + 128 * cg * 2 + (lane >> 4) * 1024 + (lane & 15) * 16;
#pragma unroll
      for (int t = 0; t < 2; ++t)
#pragma unroll
        for (int i = 0; i < 4; ++i) WO[t][i] = ldnt(wob + t * 16384 + i * 4096);
    }
    SB();
    if (w >= 4) {
      const int t = (w - 4) >> 1, cb = w & 1, rb = rb0 + t;
      const unsigned mo = WS_M + (((cb * 32 + (rb >> 3)) * 2 + (rb & 1)) * 32 + (lane & 31)) * 16;
      const unsigned pb = WS_P + (cb * 256 + rb) * 2048 + lane * 16;
      unsigned spins = 0;
      v4u gm;
      while (true) {
        CBAR();
        gm = ld_sc1(rs, mo);
        if (!__any(g16bad(gm, tag) != 0u)) break;
        if (++spins > SPIN_CAP) break;
      }
      const float m = gv(gm.x);
      float M = fmaxf(m, dpp<XOR1>(m)); M = fmaxf(M, dpp<XOR2>(M)); M = fmaxf(M, dpp<ROR4>(M)); M = fmaxf(M, dpp<ROR8>(M)); M = x16_max(M);
      float e = __builtin_amdgcn_exp2f((m - M) * LOG2E);
      float Ls = e * gv(gm.y);
      Ls += dpp<XOR1>(Ls); Ls += dpp<XOR2>(Ls); Ls += dpp<ROR4>(Ls); Ls += dpp<ROR8>(Ls); Ls = x16_sum(Ls);
      e *= __builtin_amdgcn_rcpf(Ls);
      const float e0 = __shfl(e, lane >> 2), e1 = __shfl(e, 16 + (lane >> 2));
      v4u g0_, g1_;
      while (true) {
        CBAR();
        g0_ = ld_sc1(rs, pb); g1_ = ld_sc1(rs, pb + 1024);
        if (!__any((g16bad(g0_, tag) | g16bad(g1_, tag)) != 0u)) break;
        if (++spins > SPIN_CAP) break;
      }
      STAMP(8);
      const float o0 = fmaf(e0, gv(g0_.x), e1 * gv(g1_.x)), o1 = fmaf(e0, gv(g0_.y), e1 * gv(g1_.y)), o2 = fmaf(e0, gv(g0_.z), e1 * gv(g1_.z)), o3 = fmaf(e0, gv(g0_.w), e1 * gv(g1_.w));
      const float t0 = x32_tr(o0, o1), t1 = x32_tr(o2, o3);
      float u_ = x16_tr(t0, t1);
      u_ += dpp<ROR4>(u_); u_ += dpp<ROR8>(u_);
      const unsigned ob = cvtpk(u_, u_);
      if ((lane & 12) == 0) ((LAS unsigned short*)(L + L_OS))[(t * 2 + cb) * 16 + 4 * (lane & 3) + 2 * ((lane >> 4) & 1) + (lane >> 5)] = (unsigned short)ob;
    }
    __syncthreads();
#pragma unroll
    for (int t = 0; t < 2; ++t) {
      f32x4 c1 = {0.f, 0.f, 0.f, 0.f}, c2 = {0.f, 0.f, 0.f, 0.f};
#pragma unroll
      for (int i = 0; i < 4; ++i) {
        const unsigned tt = ((const LAS unsigned*)(L + L_OS))[(t * 2 + isB) * 8 + 2 * i + (gq >> 1)];
        const unsigned oq = (gq & 1) ? ((tt >> 16) | (tt & 0xffff0000u)) : ((tt << 16) | (tt & 0xffffu));
        const v2u pa = {oq & pm0, oq & pm1};
        c1 = mfma16(pa, v2u{WO[t][i].x, WO[t][i].y}, c1); c2 = mfma16(pa, v2u{WO[t][i].z, WO[t][i].w}, c2);
      }
      const f32x4 c = (gq & 1) ? c2 : c1;
      const int col = 128 * cg + 8 * (lane & 15) + 4 * (gq & 1);
      st_sc1(rs, gq < 2 ? WS_OP + ((isB * 32 + (col >> 4)) * 256 + rb0 + t) * 64 + (col & 15) * 4 : 0x7ffffff0u, g16pack(c[0], c[1], c[2], c[3], tag));
    }
  }
  STAMP(9);
  if (b < 64) {
    const unsigned fo = WS_OP + b * 16384 + tid * 16;
    v4u g0_, g1_;
    unsigned spins = 0;
    while (true) {
      CBAR();
      g0_ = ld_sc1(rs, fo); g1_ = ld_sc1(rs, fo + 8192);
      if (!__any((g16bad(g0_, tag) | g16bad(g1_, tag)) != 0u)) break;
      if (++spins > SPIN_CAP) break;
    }
    STAMP(10);
    float s[4] = {gv(g0_.x) + gv(g1_.x), gv(g0_.y) + gv(g1_.y), gv(g0_.z) + gv(g1_.z), gv(g0_.w) + gv(g1_.w)};
#pragma unroll
    for (int k = 0; k < 4; ++k) { s[k] += dpp<ROR4>(s[k]); s[k] += dpp<ROR8>(s[k]); s[k] = x16_sum(s[k]); s[k] = x32_sum(s[k]); }
    if (lane < 4) *(LAS f32x4*)((LAS float*)(L + L_FIN) + w * 16 + 4 * lane) = f32x4{s[0], s[1], s[2], s[3]};
    __syncthreads();
    if (tid < 8) {
      float t0 = 0.f, t1 = 0.f;
#pragma unroll
      for (int ww = 0; ww < 8; ++ww) { const f32x2 t = *(const LAS f32x2*)((const LAS float*)(L + L_FIN) + ww * 16 + 2 * tid); t0 += t[0]; t1 += t[1]; }
      *(GAS unsigned*)(p.out + (b >> 5) * 512 + (b & 31) * 16 + 2 * tid) = cvtpk(t0, t1);
    }
    if (b == 0 && tid == 0) *(GAS unsigned long long*)(p.ws + WS_EPOCH) = E + 1;
  }
  STAMP(11);
}

extern "C" void kernel_launch(void* const* d_in, const int* in_sizes, int n_in,
                              void* d_out, int out_size, void* d_ws, size_t ws_size,
                              hipStream_t stream) {
  Params p;
  p.x[0] = (const unsigned short*)d_in[0]; p.k[0] = (const unsigned char*)d_in[1]; p.v[0] = (const unsigned char*)d_in[2];
  p.wq[0] = (const unsigned char*)d_in[3]; p.wo[0] = (const unsigned char*)d_in[4];
  p.x[1] = (const unsigned short*)d_in[5]; p.k[1] = (const unsigned char*)d_in[6]; p.v[1] = (const unsigned char*)d_in[7];
  p.wq[1] = (const unsigned char*)d_in[8]; p.wo[1] = (const unsigned char*)d_in[9];
  p.out = (unsigned short*)d_out; p.ws = (unsigned char*)d_ws;
  fused_decode<<<NWG, NTHR, 0, stream>>>(p);
}
```

```cpp
#include <hip/hip_runtime.h>
#include <stdint.h>

#define GAS __attribute__((address_space(1)))
#define LAS __attribute__((address_space(3)))
typedef unsigned v4u __attribute__((ext_vector_type(4)));
typedef unsigned v2u __attribute__((ext_vector_type(2)));
typedef float f32x4 __attribute__((ext_vector_type(4)));
typedef float f32x2 __attribute__((ext_vector_type(2)));
typedef __bf16 bf16x8 __attribute__((ext_vector_type(8)));
typedef __bf16 bf16x2 __attribute__((ext_vector_type(2)));
typedef short s16x4 __attribute__((ext_vector_type(4)));

#define STAMPS 0

constexpr int SK = 32768, WIN = 4096;
constexpr int NWG = 256, NTHR = 512;
constexpr float SCALE = 0.08838834764831845f;
constexpr float LOG2E = 1.4426950408889634f;

constexpr unsigned WS_EPOCH = 0;
constexpr unsigned WS_QP = 4096;
constexpr unsigned WS_P = WS_QP + 2 * 64 * 2 * 512;
constexpr unsigned WS_M = WS_P + 2 * 256 * 32 * 64;
constexpr unsigned WS_OP = WS_M + 2 * 32 * 2 * 32 * 16;
constexpr unsigned WS_END = WS_OP + 2 * 32 * 256 * 64;
constexpr unsigned WS_STAMP = (WS_END + 4095) / 4096 * 4096;

constexpr int REC_BYTES = 8 * 512 * 4;
constexpr int L_RED = 0;
constexpr int L_QS = L_RED + 2048;
constexpr int L_OS = L_QS + 2048;
constexpr int L_FIN = L_OS + 64;
constexpr int L_MX = L_FIN + 512;
constexpr int L_LW = L_MX + 256;
constexpr int L_MA = L_LW + 256;
constexpr int L_USED = L_MA + 2 * REC_BYTES;
constexpr int L_END = L_USED > 84 * 1024 ? L_USED : 84 * 1024;

constexpr unsigned SPIN_CAP = 1u << 18;

struct Params {
  const unsigned short* x[2];
  const unsigned char* k[2];
  const unsigned char* v[2];
  const unsigned char* wq[2];
  const unsigned char* wo[2];
  unsigned short* out;
  unsigned char* ws;
};

template <int CTRL> __device__ __forceinline__ float dpp(float x) {
  return __builtin_bit_cast(float, __builtin_amdgcn_mov_dpp(__builtin_bit_cast(int, x), CTRL, 0xf, 0xf, true));
}
constexpr int XOR1 = 0xB1, XOR2 = 0x4E, ROR4 = 0x124, ROR8 = 0x128, BCAST = 0x150;
__device__ __forceinline__ unsigned cvtpk(float lo, float hi) {
  f32x2 v = {lo, hi};
  return __builtin_bit_cast(unsigned, __builtin_convertvector(v, bf16x2));
}
__device__ __forceinline__ float bflo(unsigned u) { return __uint_as_float(u << 16); }
__device__ __forceinline__ float bfhi(unsigned u) { return __uint_as_float(u & 0xffff0000u); }
__device__ __forceinline__ float x16_sum(float x) {
  auto s = __builtin_amdgcn_permlane16_swap(__float_as_uint(x), __float_as_uint(x), false, false);
  return __uint_as_float(s[0]) + __uint_as_float(s[1]);
}
__device__ __forceinline__ float x32_sum(float x) {
  auto s = __builtin_amdgcn_permlane32_swap(__float_as_uint(x), __float_as_uint(x), false, false);
  return __uint_as_float(s[0]) + __uint_as_float(s[1]);
}
__device__ __forceinline__ float x32_tr(float a, float b) {
  auto s = __builtin_amdgcn_permlane32_swap(__float_as_uint(a), __float_as_uint(b), false, false);
  return __uint_as_float(s[0]) + __uint_as_float(s[1]);
}
__device__ __forceinline__ float x16_tr(float a, float b) {
  auto s = __builtin_amdgcn_permlane16_swap(__float_as_uint(a), __float_as_uint(b), false, false);
  return __uint_as_float(s[0]) + __uint_as_float(s[1]);
}
__device__ __forceinline__ float x16_max(float x) {
  auto s = __builtin_amdgcn_permlane16_swap(__float_as_uint(x), __float_as_uint(x), false, false);
  return fmaxf(__uint_as_float(s[0]), __uint_as_float(s[1]));
}
__device__ __forceinline__ float x32_max(float x) {
  auto s = __builtin_amdgcn_permlane32_swap(__float_as_uint(x), __float_as_uint(x), false, false);
  return fmaxf(__uint_as_float(s[0]), __uint_as_float(s[1]));
}
__device__ __forceinline__ v4u ldnt(const unsigned char* p) { return __builtin_nontemporal_load((const GAS v4u*)p); }
#define SB() __builtin_amdgcn_sched_barrier(0)
#define USE(x) do { asm volatile("" :: "v"(x)); } while (0)
#define CBAR() asm volatile("" ::: "memory")

__device__ __forceinline__ f32x4 mfma32(v4u a, v4u b, f32x4 c) {
  return __builtin_amdgcn_mfma_f32_16x16x32_bf16(__builtin_bit_cast(bf16x8, a), __builtin_bit_cast(bf16x8, b), c, 0, 0, 0);
}
__device__ __forceinline__ f32x4 mfma16(v2u a, v2u b, f32x4 c) {
  return __builtin_amdgcn_mfma_f32_16x16x16bf16_1k(__builtin_bit_cast(s16x4, a), __builtin_bit_cast(s16x4, b), c, 0, 0, 0);
}
__device__ __forceinline__ float qk_group(v4u kf, v4u qf) {
  f32x4 z = {0.f, 0.f, 0.f, 0.f};
  f32x4 d = mfma32(kf, qf, z);
  float s = dpp<0x00>(d[0]) + dpp<0x55>(d[1]);
  s += dpp<0xAA>(d[2]);
  s += dpp<0xFF>(d[3]);
  return s;
}
__device__ __forceinline__ void round_pair(float& a, float& b) {
  unsigned u = cvtpk(a, b);
  float a1 = bflo(u) * SCALE, b1 = bfhi(u) * SCALE;
  unsigned w = cvtpk(a1, b1);
  a = bflo(w); b = bfhi(w);
}

struct Tag { unsigned lo, hi8; v4u t; };
__device__ __forceinline__ unsigned g8pack(float f, unsigned hi8) { return ((__float_as_uint(f) + 0x80u) & 0xffffff00u) | hi8; }
__device__ __forceinline__ float g8val(unsigned hi) { return __uint_as_float(hi & 0xffffff00u); }
__device__ __forceinline__ unsigned g8bad(v4u g, const Tag& t) { return (g.x ^ t.lo) | (g.z ^ t.lo) | (((g.y ^ t.hi8) | (g.w ^ t.hi8)) & 0xffu); }
__device__ __forceinline__ unsigned gw(float f, unsigned t10) { return ((__float_as_uint(f) + 0x200u) & 0xfffffc00u) | t10; }
__device__ __forceinline__ v4u g16pack(float a, float b, float c, float d, const Tag& t) { return v4u{gw(a, t.t.x), gw(b, t.t.y), gw(c, t.t.z), gw(d, t.t.w)}; }
__device__ __forceinline__ float gv(unsigned w) { return __uint_as_float(w & 0xfffffc00u); }
__device__ __forceinline__ unsigned g16bad(v4u g, const Tag& t) { return ((g.x ^ t.t.x) | (g.y ^ t.t.y) | (g.z ^ t.t.z) | (g.w ^ t.t.w)) & 0x3ffu; }

__device__ __forceinline__ v4u ld_sc1(__amdgpu_buffer_rsrc_t rs, unsigned off) { return __builtin_amdgcn_raw_buffer_load_b128(rs, off, 0, 16); }
__device__ __forceinline__ void st_sc1(__amdgpu_buffer_rsrc_t rs, unsigned off, v4u v) { __builtin_amdgcn_raw_buffer_store_b128(v, rs, off, 0, 16); }

#define STAMP(i) do {} while (0)

__device__ __forceinline__ void write_rec(LAS unsigned char* base, int w, int lane, f32x4 a1, f32x4 a2) {
  LAS float* rec = (LAS float*)(base) + w * 512 + (lane >> 4) * 128 + (lane & 15) * 8;
  *(LAS f32x4*)rec = a1;
  *(LAS f32x4*)(rec + 4) = a2;
}
__device__ __forceinline__ void merge_publish(const LAS unsigned char* base, int hp, int lane, __amdgpu_buffer_rsrc_t rs, int br, int hk, int slot, const Tag& tag) {
  const int h = 2 * hp + (lane >> 5), d = 4 * (lane & 31);
  const LAS float* rec = (const LAS float*)base + h * 128 + d;
  f32x4 ov[8];
#pragma unroll
  for (int w = 0; w < 8; ++w) ov[w] = *(const LAS f32x4*)(rec + w * 512);
  const f32x4 o = ((ov[0] + ov[1]) + (ov[2] + ov[3])) + ((ov[4] + ov[5]) + (ov[6] + ov[7]));
  const int hq = hk * 4 + h;
  st_sc1(rs, WS_P + (((br * 256 + hq * 8 + (d >> 4)) * 32 + slot) * 64) + (d & 15) * 4, g16pack(o[0], o[1], o[2], o[3], tag));
}

__global__ void __launch_bounds__(NTHR, 2) fused_decode(Params p) {
  __shared__ __attribute__((aligned(16))) unsigned char smem_[L_END];
  LAS unsigned char* L = (LAS unsigned char*)smem_;
  const int tid = threadIdx.x, lane = tid & 63, w = __builtin_amdgcn_readfirstlane(tid >> 6), b = blockIdx.x;
  const int h = b & 7, j = b >> 3;
  const __amdgpu_buffer_rsrc_t rs = __builtin_amdgcn_make_buffer_rsrc((void*)p.ws, 0, (int)WS_END, 0x00020000);
  STAMP(0);

  const int qbr = b >> 7, qcb = (b >> 1) & 63, qhalf = b & 1;
  const int qrow0 = 256 * qhalf + 32 * w + 4 * (lane >> 3);
  const v2u xv = *(const GAS v2u*)(p.x[qbr] + qrow0);
  v4u wqr[4];
  {
    const unsigned char* wp = p.wq[qbr] + (size_t)qrow0 * 8192 + (64 * qcb + 8 * (lane & 7)) * 2;
#pragma unroll
    for (int i = 0; i < 4; ++i) wqr[i] = ldnt(wp + (size_t)i * 8192);
  }
  SB();
  __builtin_amdgcn_s_barrier();
  SB();
  const unsigned long long E = *(const GAS unsigned long long*)(p.ws + WS_EPOCH);
  Tag tag;
  {
    unsigned long long z = E + 0x9E3779B97F4A7C15ull;
    z = (z ^ (z >> 30)) * 0xBF58476D1CE4E5B9ull; z = (z ^ (z >> 27)) * 0x94D049BB133111EBull; z ^= z >> 31;
    tag.lo = (unsigned)z; tag.hi8 = (unsigned)(z >> 32) & 0xffu;
    tag.t = v4u{(unsigned)z & 0x3ffu, (unsigned)(z >> 10) & 0x3ffu, (unsigned)(z >> 20) & 0x3ffu, (unsigned)(z >> 30) & 0x3ffu};
  }
  const unsigned koff = ((lane & 15) >> 2) * 256 + (lane >> 4) * 64 + (lane & 3) * 16;
  const unsigned voff = lane * 16;
  const size_t rowA = (size_t)h * SK + 1024 * j + 16 * w;
  const unsigned char* kA = p.k[0] + rowA * 256;
  const unsigned char* vA = p.v[0] + rowA * 256;
  const size_t rowB = (size_t)h * SK + (SK - WIN) + 128 * j + 16 * w;
  const unsigned char* kB = p.k[1] + rowB * 256;
  const unsigned char* vB = p.v[1] + rowB * 256;
  v4u K[9][4], V[9][4], WO[4];
#define KADDR(u) (((u) < 16 ? kA + ((u) >> 1) * 32768 : kB) + (2 * ((u) & 1)) * 1024 + koff)
#define VADDR(u) (((u) < 16 ? vA + ((u) >> 1) * 32768 : vB) + (2 * ((u) & 1)) * 1024 + voff)
#define ISSUE_KU(u) do { K[(u) >> 1][2 * ((u) & 1)] = ldnt(KADDR(u)); K[(u) >> 1][2 * ((u) & 1) + 1] = ldnt(KADDR(u) + 1024); SB(); } while (0)
#define ISSUE_VU(u) do { V[(u) >> 1][2 * ((u) & 1)] = ldnt(VADDR(u)); V[(u) >> 1][2 * ((u) & 1) + 1] = ldnt(VADDR(u) + 1024); SB(); } while (0)
#define USE_KU(u) USE(K[(u) >> 1][2 * ((u) & 1) + 1])
#define USE_VU(u) USE(V[(u) >> 1][2 * ((u) & 1) + 1])
  ISSUE_KU(0); ISSUE_KU(1);

  {
    float acc[8];
#pragma unroll
    for (int c = 0; c < 8; ++c) acc[c] = 0.f;
    const float xs[4] = {bflo(xv.x), bfhi(xv.x), bflo(xv.y), bfhi(xv.y)};
#pragma unroll
    for (int i = 0; i < 4; ++i) {
#pragma unroll
      for (int c = 0; c < 4; ++c) { acc[2 * c] = fmaf(xs[i], bflo(wqr[i][c]), acc[2 * c]); acc[2 * c + 1] = fmaf(xs[i], bfhi(wqr[i][c]), acc[2 * c + 1]); }
    }
#pragma unroll
    for (int c = 0; c < 8; ++c) { float s = acc[c]; s += dpp<ROR8>(s); s = x16_sum(s); s = x32_sum(s); acc[c] = s; }
    if (lane < 8) {
      LAS float* r = (LAS float*)(L + L_RED) + w * 64 + lane * 8;
      *(LAS f32x4*)r = f32x4{acc[0], acc[1], acc[2], acc[3]};
      *(LAS f32x4*)(r + 4) = f32x4{acc[4], acc[5], acc[6], acc[7]};
    }
  }
  STAMP(1);
  __syncthreads();
  if (w == 0 && lane < 32) {
    const LAS float* r = (const LAS float*)(L + L_RED) + 2 * lane;
    float s0 = 0.f, s1 = 0.f;
#pragma unroll
    for (int ww = 0; ww < 8; ++ww) { const f32x2 t = *(const LAS f32x2*)(r + ww * 64); s0 += t[0]; s1 += t[1]; }
    v4u g = {tag.lo, g8pack(s0, tag.hi8), tag.lo, g8pack(s1, tag.hi8)};
    st_sc1(rs, WS_QP + ((qbr * 64 + qcb) * 2 + qhalf) * 512 + lane * 16, g);
  }
  SB();
  USE_KU(0); ISSUE_KU(2);
  USE_KU(1); ISSUE_KU(3);
  USE_KU(2); ISSUE_KU(4);
  USE_KU(3); ISSUE_KU(5);
  const unsigned goff = WS_QP + (((w >> 2) * 64 + 8 * h + 2 * (w & 3) + (lane >> 5)) * 2) * 512 + (lane & 31) * 16;
  v4u g0 = ld_sc1(rs, goff), g1 = ld_sc1(rs, goff + 512);
  SB();
  USE_KU(4); ISSUE_KU(6);
  USE_KU(5); ISSUE_KU(7);
  {
    unsigned spins = 0;
    while (__any((g8bad(g0, tag) | g8bad(g1, tag)) != 0u)) {
      if (++spins > SPIN_CAP) break;
      __builtin_amdgcn_s_sleep(2);
      g0 = ld_sc1(rs, goff); g1 = ld_sc1(rs, goff + 512);
    }
    const float q0 = g8val(g0.y) + g8val(g1.y), q1 = g8val(g0.w) + g8val(g1.w);
    *(LAS unsigned*)(L + L_QS + (w >> 2) * 1024 + ((2 * (w & 3) + (lane >> 5)) * 64 + 2 * (lane & 31)) * 2) = cvtpk(q0, q1);
  }
  SB();
  USE_KU(6); ISSUE_KU(8);
  STAMP(2);
  __syncthreads();
  STAMP(3);
  const unsigned qldsoff = ((lane & 15) >> 2) * 256 + (4 * (lane >> 4) + (lane & 3)) * 16;
  const v4u qA = *(const LAS v4u*)(L + L_QS + qldsoff);
  const unsigned pm0 = (lane & 3) == 0 ? 0xffffu : (lane & 3) == 1 ? 0xffff0000u : 0u;
  const unsigned pm1 = (lane & 3) == 2 ? 0xffffu : (lane & 3) == 3 ? 0xffff0000u : 0u;

  float sc[9][4];
#define QK_TILE(t, q_) do { _Pragma("unroll") for (int i_ = 0; i_ < 4; ++i_) sc[t][i_] = qk_group(K[t][i_], q_); \
                        round_pair(sc[t][0], sc[t][1]); round_pair(sc[t][2], sc[t][3]); SB(); } while (0)
  QK_TILE(0, qA); USE_KU(7); ISSUE_KU(9);
  QK_TILE(1, qA); USE_KU(8); ISSUE_KU(10);
  QK_TILE(2, qA); USE_KU(9); ISSUE_KU(11);
  QK_TILE(3, qA); USE_KU(10); ISSUE_KU(12);
  USE_KU(11); ISSUE_KU(13);
  QK_TILE(4, qA); USE_KU(12); ISSUE_KU(14);
  QK_TILE(5, qA); USE_KU(13); ISSUE_KU(15);
  USE_KU(14); ISSUE_KU(16);
  QK_TILE(6, qA); USE_KU(15); ISSUE_KU(17);
  QK_TILE(7, qA); USE_KU(16); ISSUE_VU(0);
  const v4u qB = *(const LAS v4u*)(L + L_QS + 1024 + qldsoff);
  USE_KU(17); ISSUE_VU(1);
  QK_TILE(8, qB);
  {
    float mA = -INFINITY;
#pragma unroll
    for (int t = 0; t < 8; ++t) mA = fmaxf(mA, fmaxf(fmaxf(sc[t][0], sc[t][1]), fmaxf(sc[t][2], sc[t][3])));
    float mB = fmaxf(fmaxf(sc[8][0], sc[8][1]), fmaxf(sc[8][2], sc[8][3]));
    mA = x16_max(mA); mA = x32_max(mA); mB = x16_max(mB); mB = x32_max(mB);
    if (lane < 16 && (lane & 3) == 0) { LAS float* mx = (LAS float*)(L + L_MX) + (lane >> 2) * 8 + w; mx[0] = mA; mx[32] = mB; }
  }
  __syncthreads();
  float MA, MB;
  {
    const LAS float* mx = (const LAS float*)(L + L_MX) + ((lane & 15) >> 2) * 8;
    const f32x4 x0 = *(const LAS f32x4*)mx, x1 = *(const LAS f32x4*)(mx + 4), y0 = *(const LAS f32x4*)(mx + 32), y1 = *(const LAS f32x4*)(mx + 36);
    MA = fmaxf(fmaxf(fmaxf(x0[0], x0[1]), fmaxf(x0[2], x0[3])), fmaxf(fmaxf(x1[0], x1[1]), fmaxf(x1[2], x1[3])));
    MB = fmaxf(fmaxf(fmaxf(y0[0], y0[1]), fmaxf(y0[2], y0[3])), fmaxf(fmaxf(y1[0], y1[1]), fmaxf(y1[2], y1[3])));
  }
  unsigned pp[9][4];
  {
    float lA = 0.f, lB = 0.f;
    const float mbA = -MA * LOG2E, mbB = -MB * LOG2E;
#pragma unroll
    for (int t = 0; t < 9; ++t) {
#pragma unroll
      for (int i = 0; i < 4; ++i) { const float pe = __builtin_amdgcn_exp2f(fmaf(sc[t][i], LOG2E, t < 8 ? mbA : mbB)); if (t < 8) lA += pe; else lB += pe; pp[t][i] = cvtpk(pe, pe); }
      SB();
      if (t == 3) { USE_VU(0); ISSUE_VU(2); }
    }
    lA = x16_sum(lA); lA = x32_sum(lA); lB = x16_sum(lB); lB = x32_sum(lB);
    if (lane < 16 && (lane & 3) == 0) { LAS float* lw = (LAS float*)(L + L_LW) + (lane >> 2) * 8 + w; lw[0] = lA; lw[32] = lB; }
  }
  SB();
  STAMP(4);
  const unsigned wooff = (lane >> 4) * 1024 + (lane & 15) * 16;
  const unsigned char* wob = p.wo[w >> 2] + (size_t)(16 * b) * 1024 + 128 * (w & 3) * 2;
#define ISSUE_WO(half) do { WO[2 * (half)] = ldnt(wob + (half) * 8192 + wooff); WO[2 * (half) + 1] = ldnt(wob + (half) * 8192 + 4096 + wooff); SB(); } while (0)
  const int isB = w >> 2, cg = w & 3, gq = lane >> 4;
  const unsigned mo = WS_M + ((((w & 1) * 32 + (b >> 3)) * 2 + (b & 1)) * 32 + (lane & 31)) * 16;
  v4u gm = {0u, 0u, 0u, 0u};
  f32x4 a1 = {0.f, 0.f, 0.f, 0.f}, a2 = {0.f, 0.f, 0.f, 0.f}, b1 = {0.f, 0.f, 0.f, 0.f}, b2 = {0.f, 0.f, 0.f, 0.f};
#pragma unroll
  for (int u = 0; u < 18; ++u) {
    const int t = u >> 1;
#pragma unroll
    for (int i = 2 * (u & 1); i < 2 * (u & 1) + 2; ++i) {
      const v2u pa = {pp[t][i] & pm0, pp[t][i] & pm1};
      if (u < 16) { a1 = mfma16(pa, v2u{V[t][i].x, V[t][i].y}, a1); a2 = mfma16(pa, v2u{V[t][i].z, V[t][i].w}, a2); }
      else { b1 = mfma16(pa, v2u{V[t][i].x, V[t][i].y}, b1); b2 = mfma16(pa, v2u{V[t][i].z, V[t][i].w}, b2); }
    }
    SB();
    if (u == 2) {
      __syncthreads();
      float Lt = 0.f;
      const LAS float* lw = (const LAS float*)(L + L_LW) + (lane & 7) * 8;
      if (w == 7) { const f32x4 x0 = *(const LAS f32x4*)lw, x1 = *(const LAS f32x4*)(lw + 4); Lt = ((x0[0] + x0[1]) + (x0[2] + x0[3])) + ((x1[0] + x1[1]) + (x1[2] + x1[3])); }
      const float Ma_ = __shfl(MA, 4 * (lane & 3)), Mb_ = __shfl(MB, 4 * (lane & 3));
      const float Mt = (lane & 4) ? Mb_ : Ma_;
      st_sc1(rs, (w == 7 && lane < 16) ? WS_M + (((((lane >> 2) & 1) * 32 + h * 4 + (lane & 3)) * 2 + (lane >> 3)) * 32 + j) * 16 : 0x7ffffff0u, g16pack(Mt, Lt, Mt, Lt, tag));
      SB();
    }
    if (u == 11) { gm = ld_sc1(rs, mo); SB(); }
    if (u == 15) write_rec(L + L_MA, w, lane, a1, a2);
    if (u + 3 < 18) { ISSUE_VU(u + 3); }
  }
  write_rec(L + L_MA + REC_BYTES, w, lane, b1, b2);
  STAMP(7);
  __syncthreads();
  if (w < 4) merge_publish(L + L_MA + (w >> 1) * REC_BYTES, w & 1, lane, rs, w >> 1, h, j, tag);
  SB();
  ISSUE_WO(0); ISSUE_WO(1);
  if (w == 4 || w == 5) {
    const int cb = w - 4;
    const unsigned pb = WS_P + (cb * 256 + b) * 2048 + lane * 16;
    unsigned spins = 0;
    while (__any(g16bad(gm, tag) != 0u)) {
      if (++spins > SPIN_CAP) break;
      CBAR();
      gm = ld_sc1(rs, mo);
    }
    const float m = gv(gm.x);
    float M = fmaxf(m, dpp<XOR1>(m)); M = fmaxf(M, dpp<XOR2>(M)); M = fmaxf(M, dpp<ROR4>(M)); M = fmaxf(M, dpp<ROR8>(M)); M = x16_max(M);
    float e = __builtin_amdgcn_exp2f((m - M) * LOG2E);
    float Ls = e * gv(gm.y);
    Ls += dpp<XOR1>(Ls); Ls += dpp<XOR2>(Ls); Ls += dpp<ROR4>(Ls); Ls += dpp<ROR8>(Ls); Ls = x16_sum(Ls);
    e *= __builtin_amdgcn_rcpf(Ls);
    const float e0 = __shfl(e, lane >> 2), e1 = __shfl(e, 16 + (lane >> 2));
    v4u g0_, g1_;
    while (true) {
      CBAR();
      g0_ = ld_sc1(rs, pb); g1_ = ld_sc1(rs, pb + 1024);
      if (!__any((g16bad(g0_, tag) | g16bad(g1_, tag)) != 0u)) break;
      if (++spins > SPIN_CAP) break;
    }
    STAMP(8);
    const float o0 = fmaf(e0, gv(g0_.x), e1 * gv(g1_.x)), o1 = fmaf(e0, gv(g0_.y), e1 * gv(g1_.y)), o2 = fmaf(e0, gv(g0_.z), e1 * gv(g1_.z)), o3 = fmaf(e0, gv(g0_.w), e1 * gv(g1_.w));
    const float t0 = x32_tr(o0, o1), t1 = x32_tr(o2, o3);
    float u_ = x16_tr(t0, t1);
    u_ += dpp<ROR4>(u_); u_ += dpp<ROR8>(u_);
    const unsigned ob = cvtpk(u_, u_);
    if ((lane & 12) == 0) ((LAS unsigned short*)(L + L_OS))[cb * 16 + 4 * (lane & 3) + 2 * ((lane >> 4) & 1) + (lane >> 5)] = (unsigned short)ob;
  }
  __syncthreads();
  {
    f32x4 c1 = {0.f, 0.f, 0.f, 0.f}, c2 = {0.f, 0.f, 0.f, 0.f};
#pragma unroll
    for (int i = 0; i < 4; ++i) {
      const unsigned t = ((const LAS unsigned*)(L + L_OS))[isB * 8 + 2 * i + (gq >> 1)];
      const unsigned oq = (gq & 1) ? ((t >> 16) | (t & 0xffff0000u)) : ((t << 16) | (t & 0xffffu));
      const v2u pa = {oq & pm0, oq & pm1};
      c1 = mfma16(pa, v2u{WO[i].x, WO[i].y}, c1); c2 = mfma16(pa, v2u{WO[i].z, WO[i].w}, c2);
    }
    const f32x4 c = (gq & 1) ? c2 : c1;
    const int col = 128 * cg + 8 * (lane & 15) + 4 * (gq & 1);
    st_sc1(rs, gq < 2 ? WS_OP + ((isB * 32 + (col >> 4)) * 256 + b) * 64 + (col & 15) * 4 : 0x7ffffff0u, g16pack(c[0], c[1], c[2], c[3], tag));
  }
  STAMP(9);
  if (b < 64) {
    const unsigned fo = WS_OP + b * 16384 + tid * 16;
    v4u g0_, g1_;
    unsigned spins = 0;
    while (true) {
      CBAR();
      g0_ = ld_sc1(rs, fo); g1_ = ld_sc1(rs, fo + 8192);
      if (!__any((g16bad(g0_, tag) | g16bad(g1_, tag)) != 0u)) break;
      if (++spins > SPIN_CAP) break;
    }
    STAMP(10);
    float s[4] = {gv(g0_.x) + gv(g1_.x), gv(g0_.y) + gv(g1_.y), gv(g0_.z) + gv(g1_.z), gv(g0_.w) + gv(g1_.w)};
#pragma unroll
    for (int k = 0; k < 4; ++k) { s[k] += dpp<ROR4>(s[k]); s[k] += dpp<ROR8>(s[k]); s[k] = x16_sum(s[k]); s[k] = x32_sum(s[k]); }
    if (lane < 4) *(LAS f32x4*)((LAS float*)(L + L_FIN) + w * 16 + 4 * lane) = f32x4{s[0], s[1], s[2], s[3]};
    __syncthreads();
    if (tid < 8) {
      float t0 = 0.f, t1 = 0.f;
#pragma unroll
      for (int ww = 0; ww < 8; ++ww) { const f32x2 t = *(const LAS f32x2*)((const LAS float*)(L + L_FIN) + ww * 16 + 2 * tid); t0 += t[0]; t1 += t[1]; }
      *(GAS unsigned*)(p.out + (b >> 5) * 512 + (b & 31) * 16 + 2 * tid) = cvtpk(t0, t1);
    }
    if (b == 0 && tid == 0) *(GAS unsigned long long*)(p.ws + WS_EPOCH) = E + 1;
  }
  STAMP(11);
}

extern "C" void kernel_launch(void* const* d_in, const int* in_sizes, int n_in,
                              void* d_out, int out_size, void* d_ws, size_t ws_size,
                              hipStream_t stream) {
  Params p;
  p.x[0] = (const unsigned short*)d_in[0]; p.k[0] = (const unsigned char*)d_in[1]; p.v[0] = (const unsigned char*)d_in[2];
  p.wq[0] = (const unsigned char*)d_in[3]; p.wo[0] = (const unsigned char*)d_in[4];
  p.x[1] = (const unsigned short*)d_in[5]; p.k[1] = (const unsigned char*)d_in[6]; p.v[1] = (const unsigned char*)d_in[7];
  p.wq[1] = (const unsigned char*)d_in[8]; p.wo[1] = (const unsigned char*)d_in[9];
  p.out = (unsigned short*)d_out; p.ws = (unsigned char*)d_ws;
  fused_decode<<<NWG, NTHR, 0, stream>>>(p);
}
```

```cpp
#include <hip/hip_runtime.h>
#include <stdint.h>

#define GAS __attribute__((address_space(1)))
#define LAS __attribute__((address_space(3)))
typedef unsigned v4u __attribute__((ext_vector_type(4)));
typedef unsigned v2u __attribute__((ext_vector_type(2)));
typedef float f32x4 __attribute__((ext_vector_type(4)));
typedef float f32x2 __attribute__((ext_vector_type(2)));
typedef __bf16 bf16x8 __attribute__((ext_vector_type(8)));
typedef __bf16 bf16x2 __attribute__((ext_vector_type(2)));
typedef short s16x4 __attribute__((ext_vector_type(4)));

constexpr int SK = 32768, WIN = 4096;
constexpr int NWG = 256, NTHR = 512;
constexpr float SCALE = 0.08838834764831845f;
constexpr float LOG2E = 1.4426950408889634f;

constexpr unsigned WS_EPOCH = 0;
constexpr unsigned WS_QP = 4096;
constexpr unsigned WS_P = WS_QP + 2 * 64 * 2 * 512;
constexpr unsigned WS_M = WS_P + 2 * 256 * 32 * 64;
constexpr unsigned WS_OP = WS_M + 2 * 32 * 2 * 32 * 16;
constexpr unsigned WS_END = WS_OP + 2 * 32 * 256 * 64;

constexpr int REC_BYTES = 8 * 512 * 4;
constexpr int L_RED = 0;
constexpr int L_QS = L_RED + 2048;
constexpr int L_OS = L_QS + 2048;
constexpr int L_FIN = L_OS + 64;
constexpr int L_MX = L_FIN + 512;
constexpr int L_LW = L_MX + 256;
constexpr int L_MA = L_LW + 256;
constexpr int L_USED = L_MA + 2 * REC_BYTES;
constexpr int L_END = L_USED > 84 * 1024 ? L_USED : 84 * 1024;

constexpr unsigned SPIN_CAP = 1u << 18;

struct Params {
  const unsigned short* x[2];
  const unsigned char* k[2];
  const unsigned char* v[2];
  const unsigned char* wq[2];
  const unsigned char* wo[2];
  unsigned short* out;
  unsigned char* ws;
};

template <int CTRL> __device__ __forceinline__ float dpp(float x) {
  return __builtin_bit_cast(float, __builtin_amdgcn_mov_dpp(__builtin_bit_cast(int, x), CTRL, 0xf, 0xf, true));
}
constexpr int XOR1 = 0xB1, XOR2 = 0x4E, ROR4 = 0x124, ROR8 = 0x128, BCAST = 0x150;
__device__ __forceinline__ unsigned cvtpk(float lo, float hi) {
  f32x2 v = {lo, hi};
  return __builtin_bit_cast(unsigned, __builtin_convertvector(v, bf16x2));
}
__device__ __forceinline__ float bflo(unsigned u) { return __uint_as_float(u << 16); }
__device__ __forceinline__ float bfhi(unsigned u) { return __uint_as_float(u & 0xffff0000u); }
__device__ __forceinline__ float x16_sum(float x) {
  auto s = __builtin_amdgcn_permlane16_swap(__float_as_uint(x), __float_as_uint(x), false, false);
  return __uint_as_float(s[0]) + __uint_as_float(s[1]);
}
__device__ __forceinline__ float x32_sum(float x) {
  auto s = __builtin_amdgcn_permlane32_swap(__float_as_uint(x), __float_as_uint(x), false, false);
  return __uint_as_float(s[0]) + __uint_as_float(s[1]);
}
__device__ __forceinline__ float x32_tr(float a, float b) {
  auto s = __builtin_amdgcn_permlane32_swap(__float_as_uint(a), __float_as_uint(b), false, false);
  return __uint_as_float(s[0]) + __uint_as_float(s[1]);
}
__device__ __forceinline__ float x16_tr(float a, float b) {
  auto s = __builtin_amdgcn_permlane16_swap(__float_as_uint(a), __float_as_uint(b), false, false);
  return __uint_as_float(s[0]) + __uint_as_float(s[1]);
}
__device__ __forceinline__ float x16_max(float x) {
  auto s = __builtin_amdgcn_permlane16_swap(__float_as_uint(x), __float_as_uint(x), false, false);
  return fmaxf(__uint_as_float(s[0]), __uint_as_float(s[1]));
}
__device__ __forceinline__ float x32_max(float x) {
  auto s = __builtin_amdgcn_permlane32_swap(__float_as_uint(x), __float_as_uint(x), false, false);
  return fmaxf(__uint_as_float(s[0]), __uint_as_float(s[1]));
}
__device__ __forceinline__ v4u ldnt(const unsigned char* p) { return *(const GAS v4u*)p; }
#define SB() __builtin_amdgcn_sched_barrier(0)
#define USE(x) do { asm volatile("" :: "v"(x)); } while (0)
#define CBAR() asm volatile("" ::: "memory")

__device__ __forceinline__ f32x4 mfma32(v4u a, v4u b, f32x4 c) {
  return __builtin_amdgcn_mfma_f32_16x16x32_bf16(__builtin_bit_cast(bf16x8, a), __builtin_bit_cast(bf16x8, b), c, 0, 0, 0);
}
__device__ __forceinline__ f32x4 mfma16(v2u a, v2u b, f32x4 c) {
  return __builtin_amdgcn_mfma_f32_16x16x16bf16_1k(__builtin_bit_cast(s16x4, a), __builtin_bit_cast(s16x4, b), c, 0, 0, 0);
}
__device__ __forceinline__ float qk_group(v4u kf, v4u qf) {
  f32x4 z = {0.f, 0.f, 0.f, 0.f};
  f32x4 d = mfma32(kf, qf, z);
  float s = dpp<0x00>(d[0]) + dpp<0x55>(d[1]);
  s += dpp<0xAA>(d[2]);
  s += dpp<0xFF>(d[3]);
  return s;
}
__device__ __forceinline__ void round_pair(float& a, float& b) {
  unsigned u = cvtpk(a, b);
  float a1 = bflo(u) * SCALE, b1 = bfhi(u) * SCALE;
  unsigned w = cvtpk(a1, b1);
  a = bflo(w); b = bfhi(w);
}

struct Tag { unsigned lo, hi8; v4u t; };
__device__ __forceinline__ unsigned g8pack(float f, unsigned hi8) { return ((__float_as_uint(f) + 0x80u) & 0xffffff00u) | hi8; }
__device__ __forceinline__ float g8val(unsigned hi) { return __uint_as_float(hi & 0xffffff00u); }
__device__ __forceinline__ unsigned g8bad(v4u g, const Tag& t) { return (g.x ^ t.lo) | (g.z ^ t.lo) | (((g.y ^ t.hi8) | (g.w ^ t.hi8)) & 0xffu); }
__device__ __forceinline__ unsigned gw(float f, unsigned t10) { return ((__float_as_uint(f) + 0x200u) & 0xfffffc00u) | t10; }
__device__ __forceinline__ v4u g16pack(float a, float b, float c, float d, const Tag& t) { return v4u{gw(a, t.t.x), gw(b, t.t.y), gw(c, t.t.z), gw(d, t.t.w)}; }
__device__ __forceinline__ float gv(unsigned w) { return __uint_as_float(w & 0xfffffc00u); }
__device__ __forceinline__ unsigned g16bad(v4u g, const Tag& t) { return ((g.x ^ t.t.x) | (g.y ^ t.t.y) | (g.z ^ t.t.z) | (g.w ^ t.t.w)) & 0x3ffu; }

__device__ __forceinline__ v4u ld_sc1(__amdgpu_buffer_rsrc_t rs, unsigned off) { return __builtin_amdgcn_raw_buffer_load_b128(rs, off, 0, 16); }
__device__ __forceinline__ void st_sc1(__amdgpu_buffer_rsrc_t rs, unsigned off, v4u v) { __builtin_amdgcn_raw_buffer_store_b128(v, rs, off, 0, 16); }

__device__ __forceinline__ void write_rec(LAS unsigned char* base, int w, int lane, f32x4 a1, f32x4 a2) {
  LAS float* rec = (LAS float*)(base) + w * 512 + (lane >> 4) * 128 + (lane & 15) * 8;
  *(LAS f32x4*)rec = a1;
  *(LAS f32x4*)(rec + 4) = a2;
}
__device__ __forceinline__ void merge_publish(const LAS unsigned char* base, int hp, int lane, __amdgpu_buffer_rsrc_t rs, int br, int hk, int slot, const Tag& tag) {
  const int h = 2 * hp + (lane >> 5), d = 4 * (lane & 31);
  const LAS float* rec = (const LAS float*)base + h * 128 + d;
  f32x4 ov[8];
#pragma unroll
  for (int w = 0; w < 8; ++w) ov[w] = *(const LAS f32x4*)(rec + w * 512);
  const f32x4 o = ((ov[0] + ov[1]) + (ov[2] + ov[3])) + ((ov[4] + ov[5]) + (ov[6] + ov[7]));
  const int hq = hk * 4 + h;
  st_sc1(rs, WS_P + (((br * 256 + hq * 8 + (d >> 4)) * 32 + slot) * 64) + (d & 15) * 4, g16pack(o[0], o[1], o[2], o[3], tag));
}

__global__ void __launch_bounds__(NTHR, 2) fused_decode(Params p) {
  __shared__ __attribute__((aligned(16))) unsigned char smem_[L_END];
  LAS unsigned char* L = (LAS unsigned char*)smem_;
  const int tid = threadIdx.x, lane = tid & 63, w = __builtin_amdgcn_readfirstlane(tid >> 6), b = blockIdx.x;
  const int h = b & 7, j = b >> 3;
  const __amdgpu_buffer_rsrc_t rs = __builtin_amdgcn_make_buffer_rsrc((void*)p.ws, 0, (int)WS_END, 0x00020000);

  const int qbr = b >> 7, qcb = (b >> 1) & 63, qhalf = b & 1;
  const int qrow0 = 256 * qhalf + 32 * w + 4 * (lane >> 3);
  const v2u xv = *(const GAS v2u*)(p.x[qbr] + qrow0);
  v4u wqr[4];
  {
    const unsigned char* wp = p.wq[qbr] + (size_t)qrow0 * 8192 + (64 * qcb + 8 * (lane & 7)) * 2;
#pragma unroll
    for (int i = 0; i < 4; ++i) wqr[i] = ldnt(wp + (size_t)i * 8192);
  }
  SB();
  __builtin_amdgcn_s_barrier();
  SB();
  const unsigned long long E = *(const GAS unsigned long long*)(p.ws + WS_EPOCH);
  Tag tag;
  {
    unsigned long long z = E + 0x9E3779B97F4A7C15ull;
    z = (z ^ (z >> 30)) * 0xBF58476D1CE4E5B9ull; z = (z ^ (z >> 27)) * 0x94D049BB133111EBull; z ^= z >> 31;
    tag.lo = (unsigned)z; tag.hi8 = (unsigned)(z >> 32) & 0xffu;
    tag.t = v4u{(unsigned)z & 0x3ffu, (unsigned)(z >> 10) & 0x3ffu, (unsigned)(z >> 20) & 0x3ffu, (unsigned)(z >> 30) & 0x3ffu};
  }
  const unsigned koff = ((lane & 15) >> 2) * 256 + (lane >> 4) * 64 + (lane & 3) * 16;
  const unsigned voff = lane * 16;
  const size_t rowA = (size_t)h * SK + 1024 * j + 16 * w;
  const unsigned char* kA = p.k[0] + rowA * 256;
  const unsigned char* vA = p.v[0] + rowA * 256;
  const size_t rowB = (size_t)h * SK + (SK - WIN) + 128 * j + 16 * w;
  const unsigned char* kB = p.k[1] + rowB * 256;
  const unsigned char* vB = p.v[1] + rowB * 256;
  v4u K[9][4], V[9][4], WO[4];
#define KADDR(u) (((u) < 16 ? kA + ((u) >> 1) * 32768 : kB) + (2 * ((u) & 1)) * 1024 + koff)
#define VADDR(u) (((u) < 16 ? vA + ((u) >> 1) * 32768 : vB) + (2 * ((u) & 1)) * 1024 + voff)
#define ISSUE_KU(u) do { K[(u) >> 1][2 * ((u) & 1)] = ldnt(KADDR(u)); K[(u) >> 1][2 * ((u) & 1) + 1] = ldnt(KADDR(u) + 1024); SB(); } while (0)
#define ISSUE_VU(u) do { V[(u) >> 1][2 * ((u) & 1)] = ldnt(VADDR(u)); V[(u) >> 1][2 * ((u) & 1) + 1] = ldnt(VADDR(u) + 1024); SB(); } while (0)
#define USE_KU(u) USE(K[(u) >> 1][2 * ((u) & 1) + 1])
#define USE_VU(u) USE(V[(u) >> 1][2 * ((u) & 1) + 1])
  ISSUE_KU(0); ISSUE_KU(1);

  {
    float acc[8];
#pragma unroll
    for (int c = 0; c < 8; ++c) acc[c] = 0.f;
    const float xs[4] = {bflo(xv.x), bfhi(xv.x), bflo(xv.y), bfhi(xv.y)};
#pragma unroll
    for (int i = 0; i < 4; ++i) {
#pragma unroll
      for (int c = 0; c < 4; ++c) { acc[2 * c] = fmaf(xs[i], bflo(wqr[i][c]), acc[2 * c]); acc[2 * c + 1] = fmaf(xs[i], bfhi(wqr[i][c]), acc[2 * c + 1]); }
    }
#pragma unroll
    for (int c = 0; c < 8; ++c) { float s = acc[c]; s += dpp<ROR8>(s); s = x16_sum(s); s = x32_sum(s); acc[c] = s; }
    if (lane < 8) {
      LAS float* r = (LAS float*)(L + L_RED) + w * 64 + lane * 8;
      *(LAS f32x4*)r = f32x4{acc[0], acc[1], acc[2], acc[3]};
      *(LAS f32x4*)(r + 4) = f32x4{acc[4], acc[5], acc[6], acc[7]};
    }
  }
  __syncthreads();
  if (w == 0 && lane < 32) {
    const LAS float* r = (const LAS float*)(L + L_RED) + 2 * lane;
    float s0 = 0.f, s1 = 0.f;
#pragma unroll
    for (int ww = 0; ww < 8; ++ww) { const f32x2 t = *(const LAS f32x2*)(r + ww * 64); s0 += t[0]; s1 += t[1]; }
    v4u g = {tag.lo, g8pack(s0, tag.hi8), tag.lo, g8pack(s1, tag.hi8)};
    st_sc1(rs, WS_QP + ((qbr * 64 + qcb) * 2 + qhalf) * 512 + lane * 16, g);
  }
  SB();
  USE_KU(0); ISSUE_KU(2);
  USE_KU(1); ISSUE_KU(3);
  USE_KU(2); ISSUE_KU(4);
  USE_KU(3); ISSUE_KU(5);
  const unsigned goff = WS_QP + (((w >> 2) * 64 + 8 * h + 2 * (w & 3) + (lane >> 5)) * 2) * 512 + (lane & 31) * 16;
  v4u g0 = ld_sc1(rs, goff), g1 = ld_sc1(rs, goff + 512);
  SB();
  USE_KU(4); ISSUE_KU(6);
  USE_KU(5); ISSUE_KU(7);
  {
    unsigned spins = 0;
    while (__any((g8bad(g0, tag) | g8bad(g1, tag)) != 0u)) {
      if (++spins > SPIN_CAP) break;
      __builtin_amdgcn_s_sleep(2);
      g0 = ld_sc1(rs, goff); g1 = ld_sc1(rs, goff + 512);
    }
    const float q0 = g8val(g0.y) + g8val(g1.y), q1 = g8val(g0.w) + g8val(g1.w);
    *(LAS unsigned*)(L + L_QS + (w >> 2) * 1024 + ((2 * (w & 3) + (lane >> 5)) * 64 + 2 * (lane & 31)) * 2) = cvtpk(q0, q1);
  }
  SB();
  USE_KU(6); ISSUE_KU(8);
  __syncthreads();
  const unsigned qldsoff = ((lane & 15) >> 2) * 256 + (4 * (lane >> 4) + (lane & 3)) * 16;
  const v4u qA = *(const LAS v4u*)(L + L_QS + qldsoff);
  const unsigned pm0 = (lane & 3) == 0 ? 0xffffu : (lane & 3) == 1 ? 0xffff0000u : 0u;
  const unsigned pm1 = (lane & 3) == 2 ? 0xffffu : (lane & 3) == 3 ? 0xffff0000u : 0u;

  float sc[9][4];
#define QK_TILE(t, q_) do { _Pragma("unroll") for (int i_ = 0; i_ < 4; ++i_) sc[t][i_] = qk_group(K[t][i_], q_); \
                        round_pair(sc[t][0], sc[t][1]); round_pair(sc[t][2], sc[t][3]); SB(); } while (0)
  QK_TILE(0, qA); USE_KU(7); ISSUE_KU(9);
  QK_TILE(1, qA); USE_KU(8); ISSUE_KU(10);
  QK_TILE(2, qA); USE_KU(9); ISSUE_KU(11);
  QK_TILE(3, qA); USE_KU(10); ISSUE_KU(12);
  USE_KU(11); ISSUE_KU(13);
  QK_TILE(4, qA); USE_KU(12); ISSUE_KU(14);
  QK_TILE(5, qA); USE_KU(13); ISSUE_KU(15);
  USE_KU(14); ISSUE_KU(16);
  QK_TILE(6, qA); USE_KU(15); ISSUE_KU(17);
  QK_TILE(7, qA); USE_KU(16); ISSUE_VU(0);
  const v4u qB = *(const LAS v4u*)(L + L_QS + 1024 + qldsoff);
  USE_KU(17); ISSUE_VU(1);
  QK_TILE(8, qB);
  {
    float mA = -INFINITY;
#pragma unroll
    for (int t = 0; t < 8; ++t) mA = fmaxf(mA, fmaxf(fmaxf(sc[t][0], sc[t][1]), fmaxf(sc[t][2], sc[t][3])));
    float mB = fmaxf(fmaxf(sc[8][0], sc[8][1]), fmaxf(sc[8][2], sc[8][3]));
    mA = x16_max(mA); mA = x32_max(mA); mB = x16_max(mB); mB = x32_max(mB);
    if (lane < 16 && (lane & 3) == 0) { LAS float* mx = (LAS float*)(L + L_MX) + (lane >> 2) * 8 + w; mx[0] = mA; mx[32] = mB; }
  }
  __syncthreads();
  float MA, MB;
  {
    const LAS float* mx = (const LAS float*)(L + L_MX) + ((lane & 15) >> 2) * 8;
    const f32x4 x0 = *(const LAS f32x4*)mx, x1 = *(const LAS f32x4*)(mx + 4), y0 = *(const LAS f32x4*)(mx + 32), y1 = *(const LAS f32x4*)(mx + 36);
    MA = fmaxf(fmaxf(fmaxf(x0[0], x0[1]), fmaxf(x0[2], x0[3])), fmaxf(fmaxf(x1[0], x1[1]), fmaxf(x1[2], x1[3])));
    MB = fmaxf(fmaxf(fmaxf(y0[0], y0[1]), fmaxf(y0[2], y0[3])), fmaxf(fmaxf(y1[0], y1[1]), fmaxf(y1[2], y1[3])));
  }
  unsigned pp[9][4];
  {
    float lA = 0.f, lB = 0.f;
    const float mbA = -MA * LOG2E, mbB = -MB * LOG2E;
#pragma unroll
    for (int t = 0; t < 9; ++t) {
#pragma unroll
      for (int i = 0; i < 4; ++i) { const float pe = __builtin_amdgcn_exp2f(fmaf(sc[t][i], LOG2E, t < 8 ? mbA : mbB)); if (t < 8) lA += pe; else lB += pe; pp[t][i] = cvtpk(pe, pe); }
      SB();
      if (t == 3) { USE_VU(0); ISSUE_VU(2); }
    }
    lA = x16_sum(lA); lA = x32_sum(lA); lB = x16_sum(lB); lB = x32_sum(lB);
    if (lane < 16 && (lane & 3) == 0) { LAS float* lw = (LAS float*)(L + L_LW) + (lane >> 2) * 8 + w; lw[0] = lA; lw[32] = lB; }
  }
  SB();
  const unsigned wooff = (lane >> 4) * 1024 + (lane & 15) * 16;
  const unsigned char* wob = p.wo[w >> 2] + (size_t)(16 * b) * 1024 + 128 * (w & 3) * 2;
#define ISSUE_WO(half) do { WO[2 * (half)] = ldnt(wob + (half) * 8192 + wooff); WO[2 * (half) + 1] = ldnt(wob + (half) * 8192 + 4096 + wooff); SB(); } while (0)
  const int isB = w >> 2, cg = w & 3, gq = lane >> 4;
  const unsigned mo = WS_M + ((((w & 1) * 32 + (b >> 3)) * 2 + (b & 1)) * 32 + (lane & 31)) * 16;
  v4u gm = {0u, 0u, 0u, 0u};
  f32x4 a1 = {0.f, 0.f, 0.f, 0.f}, a2 = {0.f, 0.f, 0.f, 0.f}, b1 = {0.f, 0.f, 0.f, 0.f}, b2 = {0.f, 0.f, 0.f, 0.f};
#pragma unroll
  for (int u = 0; u < 18; ++u) {
    const int t = u >> 1;
#pragma unroll
    for (int i = 2 * (u & 1); i < 2 * (u & 1) + 2; ++i) {
      const v2u pa = {pp[t][i] & pm0, pp[t][i] & pm1};
      if (u < 16) { a1 = mfma16(pa, v2u{V[t][i].x, V[t][i].y}, a1); a2 = mfma16(pa, v2u{V[t][i].z, V[t][i].w}, a2); }
      else { b1 = mfma16(pa, v2u{V[t][i].x, V[t][i].y}, b1); b2 = mfma16(pa, v2u{V[t][i].z, V[t][i].w}, b2); }
    }
    SB();
    if (u == 2) {
      __syncthreads();
      float Lt = 0.f;
      const LAS float* lw = (const LAS float*)(L + L_LW) + (lane & 7) * 8;
      if (w == 7) { const f32x4 x0 = *(const LAS f32x4*)lw, x1 = *(const LAS f32x4*)(lw + 4); Lt = ((x0[0] + x0[1]) + (x0[2] + x0[3])) + ((x1[0] + x1[1]) + (x1[2] + x1[3])); }
      const float Ma_ = __shfl(MA, 4 * (lane & 3)), Mb_ = __shfl(MB, 4 * (lane & 3));
      const float Mt = (lane & 4) ? Mb_ : Ma_;
      st_sc1(rs, (w == 7 && lane < 16) ? WS_M + (((((lane >> 2) & 1) * 32 + h * 4 + (lane & 3)) * 2 + (lane >> 3)) * 32 + j) * 16 : 0x7ffffff0u, g16pack(Mt, Lt, Mt, Lt, tag));
      SB();
    }
    if (u == 11) { gm = ld_sc1(rs, mo); SB(); }
    if (u == 15) write_rec(L + L_MA, w, lane, a1, a2);
    if (u + 3 < 18) { ISSUE_VU(u + 3); }
  }
  write_rec(L + L_MA + REC_BYTES, w, lane, b1, b2);
  __syncthreads();
  if (w < 4) merge_publish(L + L_MA + (w >> 1) * REC_BYTES, w & 1, lane, rs, w >> 1, h, j, tag);
  SB();
  ISSUE_WO(0); ISSUE_WO(1);
  if (w == 4 || w == 5) {
    const int cb = w - 4;
    const unsigned pb = WS_P + (cb * 256 + b) * 2048 + lane * 16;
    unsigned spins = 0;
    while (__any(g16bad(gm, tag) != 0u)) {
      if (++spins > SPIN_CAP) break;
      CBAR();
      gm = ld_sc1(rs, mo);
    }
    const float m = gv(gm.x);
    float M = fmaxf(m, dpp<XOR1>(m)); M = fmaxf(M, dpp<XOR2>(M)); M = fmaxf(M, dpp<ROR4>(M)); M = fmaxf(M, dpp<ROR8>(M)); M = x16_max(M);
    float e = __builtin_amdgcn_exp2f((m - M) * LOG2E);
    float Ls = e * gv(gm.y);
    Ls += dpp<XOR1>(Ls); Ls += dpp<XOR2>(Ls); Ls += dpp<ROR4>(Ls); Ls += dpp<ROR8>(Ls); Ls = x16_sum(Ls);
    e *= __builtin_amdgcn_rcpf(Ls);
    const float e0 = __shfl(e, lane >> 2), e1 = __shfl(e, 16 + (lane >> 2));
    v4u g0_, g1_;
    while (true) {
      CBAR();
      g0_ = ld_sc1(rs, pb); g1_ = ld_sc1(rs, pb + 1024);
      if (!__any((g16bad(g0_, tag) | g16bad(g1_, tag)) != 0u)) break;
      if (++spins > SPIN_CAP) break;
    }
    const float o0 = fmaf(e0, gv(g0_.x), e1 * gv(g1_.x)), o1 = fmaf(e0, gv(g0_.y), e1 * gv(g1_.y)), o2 = fmaf(e0, gv(g0_.z), e1 * gv(g1_.z)), o3 = fmaf(e0, gv(g0_.w), e1 * gv(g1_.w));
    const float t0 = x32_tr(o0, o1), t1 = x32_tr(o2, o3);
    float u_ = x16_tr(t0, t1);
    u_ += dpp<ROR4>(u_); u_ += dpp<ROR8>(u_);
    const unsigned ob = cvtpk(u_, u_);
    if ((lane & 12) == 0) ((LAS unsigned short*)(L + L_OS))[cb * 16 + 4 * (lane & 3) + 2 * ((lane >> 4) & 1) + (lane >> 5)] = (unsigned short)ob;
  }
  __syncthreads();
  {
    f32x4 c1 = {0.f, 0.f, 0.f, 0.f}, c2 = {0.f, 0.f, 0.f, 0.f};
#pragma unroll
    for (int i = 0; i < 4; ++i) {
      const unsigned t = ((const LAS unsigned*)(L + L_OS))[isB * 8 + 2 * i + (gq >> 1)];
      const unsigned oq = (gq & 1) ? ((t >> 16) | (t & 0xffff0000u)) : ((t << 16) | (t & 0xffffu));
      const v2u pa = {oq & pm0, oq & pm1};
      c1 = mfma16(pa, v2u{WO[i].x, WO[i].y}, c1); c2 = mfma16(pa, v2u{WO[i].z, WO[i].w}, c2);
    }
    const f32x4 c = (gq & 1) ? c2 : c1;
    const int col = 128 * cg + 8 * (lane & 15) + 4 * (gq & 1);
    st_sc1(rs, gq < 2 ? WS_OP + ((isB * 32 + (col >> 4)) * 256 + b) * 64 + (col & 15) * 4 : 0x7ffffff0u, g16pack(c[0], c[1], c[2], c[3], tag));
  }
  if (b < 64) {
    const unsigned fo = WS_OP + b * 16384 + tid * 16;
    v4u g0_, g1_;
    unsigned spins = 0;
    while (true) {
      CBAR();
      g0_ = ld_sc1(rs, fo); g1_ = ld_sc1(rs, fo + 8192);
      if (!__any((g16bad(g0_, tag) | g16bad(g1_, tag)) != 0u)) break;
      if (++spins > SPIN_CAP) break;
    }
    float s[4] = {gv(g0_.x) + gv(g1_.x), gv(g0_.y) + gv(g1_.y), gv(g0_.z) + gv(g1_.z), gv(g0_.w) + gv(g1_.w)};
#pragma unroll
    for (int k = 0; k < 4; ++k) { s[k] += dpp<ROR4>(s[k]); s[k] += dpp<ROR8>(s[k]); s[k] = x16_sum(s[k]); s[k] = x32_sum(s[k]); }
    if (lane < 4) *(LAS f32x4*)((LAS float*)(L + L_FIN) + w * 16 + 4 * lane) = f32x4{s[0], s[1], s[2], s[3]};
    __syncthreads();
    if (tid < 8) {
      float t0 = 0.f, t1 = 0.f;
#pragma unroll
      for (int ww = 0; ww < 8; ++ww) { const f32x2 t = *(const LAS f32x2*)((const LAS float*)(L + L_FIN) + ww * 16 + 2 * tid); t0 += t[0]; t1 += t[1]; }
      *(GAS unsigned*)(p.out + (b >> 5) * 512 + (b & 31) * 16 + 2 * tid) = cvtpk(t0, t1);
    }
    if (b == 0 && tid == 0) *(GAS unsigned long long*)(p.ws + WS_EPOCH) = E + 1;
  }
}

extern "C" void kernel_launch(void* const* d_in, const int* in_sizes, int n_in,
                              void* d_out, int out_size, void* d_ws, size_t ws_size,
                              hipStream_t stream) {
  Params p;
  p.x[0] = (const unsigned short*)d_in[0]; p.k[0] = (const unsigned char*)d_in[1]; p.v[0] = (const unsigned char*)d_in[2];
  p.wq[0] = (const unsigned char*)d_in[3]; p.wo[0] = (const unsigned char*)d_in[4];
  p.x[1] = (const unsigned short*)d_in[5]; p.k[1] = (const unsigned char*)d_in[6]; p.v[1] = (const unsigned char*)d_in[7];
  p.wq[1] = (const unsigned char*)d_in[8]; p.wo[1] = (const unsigned char*)d_in[9];
  p.out = (unsigned short*)d_out; p.ws = (unsigned char*)d_ws;
  fused_decode<<<NWG, NTHR, 0, stream>>>(p);
}
```

```cpp
#include <hip/hip_runtime.h>
#include <stdint.h>

#define GAS __attribute__((address_space(1)))
#define LAS __attribute__((address_space(3)))
typedef unsigned v4u __attribute__((ext_vector_type(4)));
typedef unsigned v2u __attribute__((ext_vector_type(2)));
typedef float f32x4 __attribute__((ext_vector_type(4)));
typedef float f32x2 __attribute__((ext_vector_type(2)));
typedef __bf16 bf16x8 __attribute__((ext_vector_type(8)));
typedef __bf16 bf16x2 __attribute__((ext_vector_type(2)));
typedef short s16x4 __attribute__((ext_vector_type(4)));

constexpr int SK = 32768, WIN = 4096;
constexpr int NWG = 256, NTHR = 512;
constexpr float SCALE = 0.08838834764831845f;
constexpr float LOG2E = 1.4426950408889634f;

constexpr unsigned WS_EPOCH = 0;
constexpr unsigned WS_QP = 4096;
constexpr unsigned WS_P = WS_QP + 2 * 64 * 2 * 512;
constexpr unsigned WS_M = WS_P + 2 * 256 * 32 * 64;
constexpr unsigned WS_OP = WS_M + 2 * 32 * 2 * 32 * 16;
constexpr unsigned WS_END = WS_OP + 2 * 32 * 256 * 64;

constexpr int REC_BYTES = 8 * 512 * 4;
constexpr int L_RED = 0;
constexpr int L_QS = L_RED + 2048;
constexpr int L_OS = L_QS + 2048;
constexpr int L_FIN = L_OS + 64;
constexpr int L_MX = L_FIN + 512;
constexpr int L_LW = L_MX + 256;
constexpr int L_MA = L_LW + 256;
constexpr int L_USED = L_MA + 2 * REC_BYTES;
constexpr int L_END = L_USED > 84 * 1024 ? L_USED : 84 * 1024;

constexpr unsigned SPIN_CAP = 1u << 18;

struct Params {
  const unsigned short* x[2];
  const unsigned char* k[2];
  const unsigned char* v[2];
  const unsigned char* wq[2];
  const unsigned char* wo[2];
  unsigned short* out;
  unsigned char* ws;
};

template <int CTRL> __device__ __forceinline__ float dpp(float x) {
  return __builtin_bit_cast(float, __builtin_amdgcn_mov_dpp(__builtin_bit_cast(int, x), CTRL, 0xf, 0xf, true));
}
constexpr int XOR1 = 0xB1, XOR2 = 0x4E, ROR4 = 0x124, ROR8 = 0x128, BCAST = 0x150;
__device__ __forceinline__ unsigned cvtpk(float lo, float hi) {
  f32x2 v = {lo, hi};
  return __builtin_bit_cast(unsigned, __builtin_convertvector(v, bf16x2));
}
__device__ __forceinline__ float bflo(unsigned u) { return __uint_as_float(u << 16); }
__device__ __forceinline__ float bfhi(unsigned u) { return __uint_as_float(u & 0xffff0000u); }
__device__ __forceinline__ float x16_sum(float x) {
  auto s = __builtin_amdgcn_permlane16_swap(__float_as_uint(x), __float_as_uint(x), false, false);
  return __uint_as_float(s[0]) + __uint_as_float(s[1]);
}
__device__ __forceinline__ float x32_sum(float x) {
  auto s = __builtin_amdgcn_permlane32_swap(__float_as_uint(x), __float_as_uint(x), false, false);
  return __uint_as_float(s[0]) + __uint_as_float(s[1]);
}
__device__ __forceinline__ float x32_tr(float a, float b) {
  auto s = __builtin_amdgcn_permlane32_swap(__float_as_uint(a), __float_as_uint(b), false, false);
  return __uint_as_float(s[0]) + __uint_as_float(s[1]);
}
__device__ __forceinline__ float x16_tr(float a, float b) {
  auto s = __builtin_amdgcn_permlane16_swap(__float_as_uint(a), __float_as_uint(b), false, false);
  return __uint_as_float(s[0]) + __uint_as_float(s[1]);
}
__device__ __forceinline__ float x16_max(float x) {
  auto s = __builtin_amdgcn_permlane16_swap(__float_as_uint(x), __float_as_uint(x), false, false);
  return fmaxf(__uint_as_float(s[0]), __uint_as_float(s[1]));
}
__device__ __forceinline__ float x32_max(float x) {
  auto s = __builtin_amdgcn_permlane32_swap(__float_as_uint(x), __float_as_uint(x), false, false);
  return fmaxf(__uint_as_float(s[0]), __uint_as_float(s[1]));
}
#define AUX 18
__device__ __forceinline__ __amdgpu_buffer_rsrc_t mk(const void* base) { return __builtin_amdgcn_make_buffer_rsrc((void*)base, 0, 0x7fffffff, 0x00020000); }
__device__ __forceinline__ v4u ldb(__amdgpu_buffer_rsrc_t r, unsigned voff, unsigned soff) { return __builtin_amdgcn_raw_buffer_load_b128(r, voff, soff, AUX); }
#define SB() __builtin_amdgcn_sched_barrier(0)
#define USE(x) do { asm volatile("" :: "v"(x)); } while (0)
#define CBAR() asm volatile("" ::: "memory")

__device__ __forceinline__ f32x4 mfma32(v4u a, v4u b, f32x4 c) {
  return __builtin_amdgcn_mfma_f32_16x16x32_bf16(__builtin_bit_cast(bf16x8, a), __builtin_bit_cast(bf16x8, b), c, 0, 0, 0);
}
__device__ __forceinline__ f32x4 mfma16(v2u a, v2u b, f32x4 c) {
  return __builtin_amdgcn_mfma_f32_16x16x16bf16_1k(__builtin_bit_cast(s16x4, a), __builtin_bit_cast(s16x4, b), c, 0, 0, 0);
}
__device__ __forceinline__ float qk_group(v4u kf, v4u qf) {
  f32x4 z = {0.f, 0.f, 0.f, 0.f};
  f32x4 d = mfma32(kf, qf, z);
  float s = dpp<0x00>(d[0]) + dpp<0x55>(d[1]);
  s += dpp<0xAA>(d[2]);
  s += dpp<0xFF>(d[3]);
  return s;
}
__device__ __forceinline__ void round_pair(float& a, float& b) {
  unsigned u = cvtpk(a, b);
  float a1 = bflo(u) * SCALE, b1 = bfhi(u) * SCALE;
  unsigned w = cvtpk(a1, b1);
  a = bflo(w); b = bfhi(w);
}

struct Tag { unsigned lo, hi8; v4u t; };
__device__ __forceinline__ unsigned g8pack(float f, unsigned hi8) { return ((__float_as_uint(f) + 0x80u) & 0xffffff00u) | hi8; }
__device__ __forceinline__ float g8val(unsigned hi) { return __uint_as_float(hi & 0xffffff00u); }
__device__ __forceinline__ unsigned g8bad(v4u g, const Tag& t) { return (g.x ^ t.lo) | (g.z ^ t.lo) | (((g.y ^ t.hi8) | (g.w ^ t.hi8)) & 0xffu); }
__device__ __forceinline__ unsigned gw(float f, unsigned t10) { return ((__float_as_uint(f) + 0x200u) & 0xfffffc00u) | t10; }
__device__ __forceinline__ v4u g16pack(float a, float b, float c, float d, const Tag& t) { return v4u{gw(a, t.t.x), gw(b, t.t.y), gw(c, t.t.z), gw(d, t.t.w)}; }
__device__ __forceinline__ float gv(unsigned w) { return __uint_as_float(w & 0xfffffc00u); }
__device__ __forceinline__ unsigned g16bad(v4u g, const Tag& t) { return ((g.x ^ t.t.x) | (g.y ^ t.t.y) | (g.z ^ t.t.z) | (g.w ^ t.t.w)) & 0x3ffu; }

__device__ __forceinline__ v4u ld_sc1(__amdgpu_buffer_rsrc_t rs, unsigned off) { return __builtin_amdgcn_raw_buffer_load_b128(rs, off, 0, 16); }
__device__ __forceinline__ void st_sc1(__amdgpu_buffer_rsrc_t rs, unsigned off, v4u v) { __builtin_amdgcn_raw_buffer_store_b128(v, rs, off, 0, 16); }

__device__ __forceinline__ void write_rec(LAS unsigned char* base, int w, int lane, f32x4 a1, f32x4 a2) {
  LAS float* rec = (LAS float*)(base) + w * 512 + (lane >> 4) * 128 + (lane & 15) * 8;
  *(LAS f32x4*)rec = a1;
  *(LAS f32x4*)(rec + 4) = a2;
}
__device__ __forceinline__ void merge_publish(const LAS unsigned char* base, int hp, int lane, __amdgpu_buffer_rsrc_t rs, int br, int hk, int slot, const Tag& tag) {
  const int h = 2 * hp + (lane >> 5), d = 4 * (lane & 31);
  const LAS float* rec = (const LAS float*)base + h * 128 + d;
  f32x4 ov[8];
#pragma unroll
  for (int w = 0; w < 8; ++w) ov[w] = *(const LAS f32x4*)(rec + w * 512);
  const f32x4 o = ((ov[0] + ov[1]) + (ov[2] + ov[3])) + ((ov[4] + ov[5]) + (ov[6] + ov[7]));
  const int hq = hk * 4 + h;
  st_sc1(rs, WS_P + (((br * 256 + hq * 8 + (d >> 4)) * 32 + slot) * 64) + (d & 15) * 4, g16pack(o[0], o[1], o[2], o[3], tag));
}

__global__ void __launch_bounds__(NTHR, 2) fused_decode(Params p) {
  __shared__ __attribute__((aligned(16))) unsigned char smem_[L_END];
  LAS unsigned char* L = (LAS unsigned char*)smem_;
  const int tid = threadIdx.x, lane = tid & 63, w = __builtin_amdgcn_readfirstlane(tid >> 6), b = blockIdx.x;
  const int h = b & 7, j = b >> 3;
  const __amdgpu_buffer_rsrc_t rs = __builtin_amdgcn_make_buffer_rsrc((void*)p.ws, 0, (int)WS_END, 0x00020000);

  const int qbr = b >> 7, qcb = (b >> 1) & 63, qhalf = b & 1;
  const int qrow0 = 256 * qhalf + 32 * w + 4 * (lane >> 3);
  const v2u xv = *(const GAS v2u*)(p.x[qbr] + qrow0);
  v4u wqr[4];
  {
    const __amdgpu_buffer_rsrc_t rq = mk(p.wq[qbr]);
    const unsigned wo_ = (unsigned)qrow0 * 8192u + (64 * qcb + 8 * (lane & 7)) * 2;
#pragma unroll
    for (int i = 0; i < 4; ++i) wqr[i] = ldb(rq, wo_, i * 8192);
  }
  SB();
  __builtin_amdgcn_s_barrier();
  SB();
  const unsigned long long E = *(const GAS unsigned long long*)(p.ws + WS_EPOCH);
  Tag tag;
  {
    unsigned long long z = E + 0x9E3779B97F4A7C15ull;
    z = (z ^ (z >> 30)) * 0xBF58476D1CE4E5B9ull; z = (z ^ (z >> 27)) * 0x94D049BB133111EBull; z ^= z >> 31;
    tag.lo = (unsigned)z; tag.hi8 = (unsigned)(z >> 32) & 0xffu;
    tag.t = v4u{(unsigned)z & 0x3ffu, (unsigned)(z >> 10) & 0x3ffu, (unsigned)(z >> 20) & 0x3ffu, (unsigned)(z >> 30) & 0x3ffu};
  }
  const unsigned koff = ((lane & 15) >> 2) * 256 + (lane >> 4) * 64 + (lane & 3) * 16;
  const unsigned voff = lane * 16;
  const size_t rowA = (size_t)h * SK + 1024 * j + 16 * w;
  const unsigned char* kA = p.k[0] + rowA * 256;
  const unsigned char* vA = p.v[0] + rowA * 256;
  const size_t rowB = (size_t)h * SK + (SK - WIN) + 128 * j + 16 * w;
  const unsigned char* kB = p.k[1] + rowB * 256;
  const unsigned char* vB = p.v[1] + rowB * 256;
  v4u K[9][4], V[9][4], WO[4];
  const __amdgpu_buffer_rsrc_t rkA = mk(kA), rvA = mk(vA), rkB = mk(kB), rvB = mk(vB);
#define KOFF(u) (((u) < 16 ? ((u) >> 1) * 32768 : 0) + (2 * ((u) & 1)) * 1024)
#define ISSUE_KU(u) do { K[(u) >> 1][2 * ((u) & 1)] = ldb((u) < 16 ? rkA : rkB, koff, KOFF(u)); K[(u) >> 1][2 * ((u) & 1) + 1] = ldb((u) < 16 ? rkA : rkB, koff, KOFF(u) + 1024); SB(); } while (0)
#define ISSUE_VU(u) do { V[(u) >> 1][2 * ((u) & 1)] = ldb((u) < 16 ? rvA : rvB, voff, KOFF(u)); V[(u) >> 1][2 * ((u) & 1) + 1] = ldb((u) < 16 ? rvA : rvB, voff, KOFF(u) + 1024); SB(); } while (0)
#define USE_KU(u) USE(K[(u) >> 1][2 * ((u) & 1) + 1])
#define USE_VU(u) USE(V[(u) >> 1][2 * ((u) & 1) + 1])
  ISSUE_KU(0); ISSUE_KU(1);

  {
    float acc[8];
#pragma unroll
    for (int c = 0; c < 8; ++c) acc[c] = 0.f;
    const float xs[4] = {bflo(xv.x), bfhi(xv.x), bflo(xv.y), bfhi(xv.y)};
#pragma unroll
    for (int i = 0; i < 4; ++i) {
#pragma unroll
      for (int c = 0; c < 4; ++c) { acc[2 * c] = fmaf(xs[i], bflo(wqr[i][c]), acc[2 * c]); acc[2 * c + 1] = fmaf(xs[i], bfhi(wqr[i][c]), acc[2 * c + 1]); }
    }
#pragma unroll
    for (int c = 0; c < 8; ++c) { float s = acc[c]; s += dpp<ROR8>(s); s = x16_sum(s); s = x32_sum(s); acc[c] = s; }
    if (lane < 8) {
      LAS float* r = (LAS float*)(L + L_RED) + w * 64 + lane * 8;
      *(LAS f32x4*)r = f32x4{acc[0], acc[1], acc[2], acc[3]};
      *(LAS f32x4*)(r + 4) = f32x4{acc[4], acc[5], acc[6], acc[7]};
    }
  }
  __syncthreads();
  if (w == 0 && lane < 32) {
    const LAS float* r = (const LAS float*)(L + L_RED) + 2 * lane;
    float s0 = 0.f, s1 = 0.f;
#pragma unroll
    for (int ww = 0; ww < 8; ++ww) { const f32x2 t = *(const LAS f32x2*)(r + ww * 64); s0 += t[0]; s1 += t[1]; }
    v4u g = {tag.lo, g8pack(s0, tag.hi8), tag.lo, g8pack(s1, tag.hi8)};
    st_sc1(rs, WS_QP + ((qbr * 64 + qcb) * 2 + qhalf) * 512 + lane * 16, g);
  }
  SB();
  USE_KU(0); ISSUE_KU(2);
  USE_KU(1); ISSUE_KU(3);
  USE_KU(2); ISSUE_KU(4);
  USE_KU(3); ISSUE_KU(5);
  const unsigned goff = WS_QP + (((w >> 2) * 64 + 8 * h + 2 * (w & 3) + (lane >> 5)) * 2) * 512 + (lane & 31) * 16;
  v4u g0 = ld_sc1(rs, goff), g1 = ld_sc1(rs, goff + 512);
  SB();
  USE_KU(4); ISSUE_KU(6);
  USE_KU(5); ISSUE_KU(7);
  {
    unsigned spins = 0;
    while (__any((g8bad(g0, tag) | g8bad(g1, tag)) != 0u)) {
      if (++spins > SPIN_CAP) break;
      __builtin_amdgcn_s_sleep(2);
      g0 = ld_sc1(rs, goff); g1 = ld_sc1(rs, goff + 512);
    }
    const float q0 = g8val(g0.y) + g8val(g1.y), q1 = g8val(g0.w) + g8val(g1.w);
    *(LAS unsigned*)(L + L_QS + (w >> 2) * 1024 + ((2 * (w & 3) + (lane >> 5)) * 64 + 2 * (lane & 31)) * 2) = cvtpk(q0, q1);
  }
  SB();
  USE_KU(6); ISSUE_KU(8);
  __syncthreads();
  const unsigned qldsoff = ((lane & 15) >> 2) * 256 + (4 * (lane >> 4) + (lane & 3)) * 16;
  const v4u qA = *(const LAS v4u*)(L + L_QS + qldsoff);
  const unsigned pm0 = (lane & 3) == 0 ? 0xffffu : (lane & 3) == 1 ? 0xffff0000u : 0u;
  const unsigned pm1 = (lane & 3) == 2 ? 0xffffu : (lane & 3) == 3 ? 0xffff0000u : 0u;

  float sc[9][4];
#define QK_TILE(t, q_) do { _Pragma("unroll") for (int i_ = 0; i_ < 4; ++i_) sc[t][i_] = qk_group(K[t][i_], q_); \
                        round_pair(sc[t][0], sc[t][1]); round_pair(sc[t][2], sc[t][3]); SB(); } while (0)
  QK_TILE(0, qA); USE_KU(7); ISSUE_KU(9);
  QK_TILE(1, qA); USE_KU(8); ISSUE_KU(10);
  QK_TILE(2, qA); USE_KU(9); ISSUE_KU(11);
  QK_TILE(3, qA); USE_KU(10); ISSUE_KU(12);
  USE_KU(11); ISSUE_KU(13);
  QK_TILE(4, qA); USE_KU(12); ISSUE_KU(14);
  QK_TILE(5, qA); USE_KU(13); ISSUE_KU(15);
  USE_KU(14); ISSUE_KU(16);
  QK_TILE(6, qA); USE_KU(15); ISSUE_KU(17);
  QK_TILE(7, qA); USE_KU(16); ISSUE_VU(0);
  const v4u qB = *(const LAS v4u*)(L + L_QS + 1024 + qldsoff);
  USE_KU(17); ISSUE_VU(1);
  QK_TILE(8, qB);
  {
    float mA = -INFINITY;
#pragma unroll
    for (int t = 0; t < 8; ++t) mA = fmaxf(mA, fmaxf(fmaxf(sc[t][0], sc[t][1]), fmaxf(sc[t][2], sc[t][3])));
    float mB = fmaxf(fmaxf(sc[8][0], sc[8][1]), fmaxf(sc[8][2], sc[8][3]));
    mA = x16_max(mA); mA = x32_max(mA); mB = x16_max(mB); mB = x32_max(mB);
    if (lane < 16 && (lane & 3) == 0) { LAS float* mx = (LAS float*)(L + L_MX) + (lane >> 2) * 8 + w; mx[0] = mA; mx[32] = mB; }
  }
  __syncthreads();
  float MA, MB;
  {
    const LAS float* mx = (const LAS float*)(L + L_MX) + ((lane & 15) >> 2) * 8;
    const f32x4 x0 = *(const LAS f32x4*)mx, x1 = *(const LAS f32x4*)(mx + 4), y0 = *(const LAS f32x4*)(mx + 32), y1 = *(const LAS f32x4*)(mx + 36);
    MA = fmaxf(fmaxf(fmaxf(x0[0], x0[1]), fmaxf(x0[2], x0[3])), fmaxf(fmaxf(x1[0], x1[1]), fmaxf(x1[2], x1[3])));
    MB = fmaxf(fmaxf(fmaxf(y0[0], y0[1]), fmaxf(y0[2], y0[3])), fmaxf(fmaxf(y1[0], y1[1]), fmaxf(y1[2], y1[3])));
  }
  unsigned pp[9][4];
  {
    float lA = 0.f, lB = 0.f;
    const float mbA = -MA * LOG2E, mbB = -MB * LOG2E;
#pragma unroll
    for (int t = 0; t < 9; ++t) {
#pragma unroll
      for (int i = 0; i < 4; ++i) { const float pe = __builtin_amdgcn_exp2f(fmaf(sc[t][i], LOG2E, t < 8 ? mbA : mbB)); if (t < 8) lA += pe; else lB += pe; pp[t][i] = cvtpk(pe, pe); }
      SB();
      if (t == 3) { USE_VU(0); ISSUE_VU(2); }
    }
    lA = x16_sum(lA); lA = x32_sum(lA); lB = x16_sum(lB); lB = x32_sum(lB);
    if (lane < 16 && (lane & 3) == 0) { LAS float* lw = (LAS float*)(L + L_LW) + (lane >> 2) * 8 + w; lw[0] = lA; lw[32] = lB; }
  }
  SB();
  const unsigned wooff = (lane >> 4) * 1024 + (lane & 15) * 16;
  const unsigned char* wob = p.wo[w >> 2] + (size_t)(16 * b) * 1024 + 128 * (w & 3) * 2;
  const __amdgpu_buffer_rsrc_t rwo = mk(wob);
#define ISSUE_WO(half) do { WO[2 * (half)] = ldb(rwo, wooff, (half) * 8192); WO[2 * (half) + 1] = ldb(rwo, wooff, (half) * 8192 + 4096); SB(); } while (0)
  const int isB = w >> 2, cg = w & 3, gq = lane >> 4;
  const unsigned mo = WS_M + ((((w & 1) * 32 + (b >> 3)) * 2 + (b & 1)) * 32 + (lane & 31)) * 16;
  v4u gm = {0u, 0u, 0u, 0u};
  f32x4 a1 = {0.f, 0.f, 0.f, 0.f}, a2 = {0.f, 0.f, 0.f, 0.f}, b1 = {0.f, 0.f, 0.f, 0.f}, b2 = {0.f, 0.f, 0.f, 0.f};
#pragma unroll
  for (int u = 0; u < 18; ++u) {
    const int t = u >> 1;
#pragma unroll
    for (int i = 2 * (u & 1); i < 2 * (u & 1) + 2; ++i) {
      const v2u pa = {pp[t][i] & pm0, pp[t][i] & pm1};
      if (u < 16) { a1 = mfma16(pa, v2u{V[t][i].x, V[t][i].y}, a1); a2 = mfma16(pa, v2u{V[t][i].z, V[t][i].w}, a2); }
      else { b1 = mfma16(pa, v2u{V[t][i].x, V[t][i].y}, b1); b2 = mfma16(pa, v2u{V[t][i].z, V[t][i].w}, b2); }
    }
    SB();
    if (u == 2) {
      __syncthreads();
      float Lt = 0.f;
      const LAS float* lw = (const LAS float*)(L + L_LW) + (lane & 7) * 8;
      if (w == 7) { const f32x4 x0 = *(const LAS f32x4*)lw, x1 = *(const LAS f32x4*)(lw + 4); Lt = ((x0[0] + x0[1]) + (x0[2] + x0[3])) + ((x1[0] + x1[1]) + (x1[2] + x1[3])); }
      const float Ma_ = __shfl(MA, 4 * (lane & 3)), Mb_ = __shfl(MB, 4 * (lane & 3));
      const float Mt = (lane & 4) ? Mb_ : Ma_;
      st_sc1(rs, (w == 7 && lane < 16) ? WS_M + (((((lane >> 2) & 1) * 32 + h * 4 + (lane & 3)) * 2 + (lane >> 3)) * 32 + j) * 16 : 0x7ffffff0u, g16pack(Mt, Lt, Mt, Lt, tag));
      SB();
    }
    if (u == 11) { gm = ld_sc1(rs, mo); SB(); }
    if (u == 15) write_rec(L + L_MA, w, lane, a1, a2);
    if (u + 3 < 18) { ISSUE_VU(u + 3); }
  }
  write_rec(L + L_MA + REC_BYTES, w, lane, b1, b2);
  __syncthreads();
  if (w < 4) merge_publish(L + L_MA + (w >> 1) * REC_BYTES, w & 1, lane, rs, w >> 1, h, j, tag);
  SB();
  ISSUE_WO(0); ISSUE_WO(1);
  if (w == 4 || w == 5) {
    const int cb = w - 4;
    const unsigned pb = WS_P + (cb * 256 + b) * 2048 + lane * 16;
    unsigned spins = 0;
    while (__any(g16bad(gm, tag) != 0u)) {
      if (++spins > SPIN_CAP) break;
      CBAR();
      gm = ld_sc1(rs, mo);
    }
    const float m = gv(gm.x);
    float M = fmaxf(m, dpp<XOR1>(m)); M = fmaxf(M, dpp<XOR2>(M)); M = fmaxf(M, dpp<ROR4>(M)); M = fmaxf(M, dpp<ROR8>(M)); M = x16_max(M);
    float e = __builtin_amdgcn_exp2f((m - M) * LOG2E);
    float Ls = e * gv(gm.y);
    Ls += dpp<XOR1>(Ls); Ls += dpp<XOR2>(Ls); Ls += dpp<ROR4>(Ls); Ls += dpp<ROR8>(Ls); Ls = x16_sum(Ls);
    e *= __builtin_amdgcn_rcpf(Ls);
    const float e0 = __shfl(e, lane >> 2), e1 = __shfl(e, 16 + (lane >> 2));
    v4u g0_, g1_;
    while (true) {
      CBAR();
      g0_ = ld_sc1(rs, pb); g1_ = ld_sc1(rs, pb + 1024);
      if (!__any((g16bad(g0_, tag) | g16bad(g1_, tag)) != 0u)) break;
      if (++spins > SPIN_CAP) break;
    }
    const float o0 = fmaf(e0, gv(g0_.x), e1 * gv(g1_.x)), o1 = fmaf(e0, gv(g0_.y), e1 * gv(g1_.y)), o2 = fmaf(e0, gv(g0_.z), e1 * gv(g1_.z)), o3 = fmaf(e0, gv(g0_.w), e1 * gv(g1_.w));
    const float t0 = x32_tr(o0, o1), t1 = x32_tr(o2, o3);
    float u_ = x16_tr(t0, t1);
    u_ += dpp<ROR4>(u_); u_ += dpp<ROR8>(u_);
    const unsigned ob = cvtpk(u_, u_);
    if ((lane & 12) == 0) ((LAS unsigned short*)(L + L_OS))[cb * 16 + 4 * (lane & 3) + 2 * ((lane >> 4) & 1) + (lane >> 5)] = (unsigned short)ob;
  }
  __syncthreads();
  {
    f32x4 c1 = {0.f, 0.f, 0.f, 0.f}, c2 = {0.f, 0.f, 0.f, 0.f};
#pragma unroll
    for (int i = 0; i < 4; ++i) {
      const unsigned t = ((const LAS unsigned*)(L + L_OS))[isB * 8 + 2 * i + (gq >> 1)];
      const unsigned oq = (gq & 1) ? ((t >> 16) | (t & 0xffff0000u)) : ((t << 16) | (t & 0xffffu));
      const v2u pa = {oq & pm0, oq & pm1};
      c1 = mfma16(pa, v2u{WO[i].x, WO[i].y}, c1); c2 = mfma16(pa, v2u{WO[i].z, WO[i].w}, c2);
    }
    const f32x4 c = (gq & 1) ? c2 : c1;
    const int col = 128 * cg + 8 * (lane & 15) + 4 * (gq & 1);
    st_sc1(rs, gq < 2 ? WS_OP + ((isB * 32 + (col >> 4)) * 256 + b) * 64 + (col & 15) * 4 : 0x7ffffff0u, g16pack(c[0], c[1], c[2], c[3], tag));
  }
  if (b < 64) {
    const unsigned fo = WS_OP + b * 16384 + tid * 16;
    v4u g0_, g1_;
    unsigned spins = 0;
    while (true) {
      CBAR();
      g0_ = ld_sc1(rs, fo); g1_ = ld_sc1(rs, fo + 8192);
      if (!__any((g16bad(g0_, tag) | g16bad(g1_, tag)) != 0u)) break;
      if (++spins > SPIN_CAP) break;
    }
    float s[4] = {gv(g0_.x) + gv(g1_.x), gv(g0_.y) + gv(g1_.y), gv(g0_.z) + gv(g1_.z), gv(g0_.w) + gv(g1_.w)};
#pragma unroll
    for (int k = 0; k < 4; ++k) { s[k] += dpp<ROR4>(s[k]); s[k] += dpp<ROR8>(s[k]); s[k] = x16_sum(s[k]); s[k] = x32_sum(s[k]); }
    if (lane < 4) *(LAS f32x4*)((LAS float*)(L + L_FIN) + w * 16 + 4 * lane) = f32x4{s[0], s[1], s[2], s[3]};
    __syncthreads();
    if (tid < 8) {
      float t0 = 0.f, t1 = 0.f;
#pragma unroll
      for (int ww = 0; ww < 8; ++ww) { const f32x2 t = *(const LAS f32x2*)((const LAS float*)(L + L_FIN) + ww * 16 + 2 * tid); t0 += t[0]; t1 += t[1]; }
      *(GAS unsigned*)(p.out + (b >> 5) * 512 + (b & 31) * 16 + 2 * tid) = cvtpk(t0, t1);
    }
    if (b == 0 && tid == 0) *(GAS unsigned long long*)(p.ws + WS_EPOCH) = E + 1;
  }
}

extern "C" void kernel_launch(void* const* d_in, const int* in_sizes, int n_in,
                              void* d_out, int out_size, void* d_ws, size_t ws_size,
                              hipStream_t stream) {
  Params p;
  p.x[0] = (const unsigned short*)d_in[0]; p.k[0] = (const unsigned char*)d_in[1]; p.v[0] = (const unsigned char*)d_in[2];
  p.wq[0] = (const unsigned char*)d_in[3]; p.wo[0] = (const unsigned char*)d_in[4];
  p.x[1] = (const unsigned short*)d_in[5]; p.k[1] = (const unsigned char*)d_in[6]; p.v[1] = (const unsigned char*)d_in[7];
  p.wq[1] = (const unsigned char*)d_in[8]; p.wo[1] = (const unsigned char*)d_in[9];
  p.out = (unsigned short*)d_out; p.ws = (unsigned char*)d_ws;
  fused_decode<<<NWG, NTHR, 0, stream>>>(p);
}
```

```cpp
#include <hip/hip_runtime.h>
#include <stdint.h>

#define GAS __attribute__((address_space(1)))
#define LAS __attribute__((address_space(3)))
typedef unsigned v4u __attribute__((ext_vector_type(4)));
typedef unsigned v2u __attribute__((ext_vector_type(2)));
typedef float f32x4 __attribute__((ext_vector_type(4)));
typedef float f32x2 __attribute__((ext_vector_type(2)));
typedef __bf16 bf16x8 __attribute__((ext_vector_type(8)));
typedef __bf16 bf16x2 __attribute__((ext_vector_type(2)));
typedef short s16x4 __attribute__((ext_vector_type(4)));

constexpr int SK = 32768, WIN = 4096;
constexpr int NWG = 256, NTHR = 512;
constexpr float SCALE = 0.08838834764831845f;
constexpr float LOG2E = 1.4426950408889634f;

constexpr unsigned WS_EPOCH = 0;
constexpr unsigned WS_QP = 4096;
constexpr unsigned WS_P = WS_QP + 2 * 64 * 2 * 512;
constexpr unsigned WS_M = WS_P + 2 * 256 * 32 * 64;
constexpr unsigned WS_OP = WS_M + 2 * 32 * 2 * 32 * 16;
constexpr unsigned WS_END = WS_OP + 2 * 32 * 256 * 64;

constexpr int REC_BYTES = 8 * 512 * 4;
constexpr int L_RED = 0;
constexpr int L_QS = L_RED + 2048;
constexpr int L_OS = L_QS + 2048;
constexpr int L_FIN = L_OS + 64;
constexpr int L_MX = L_FIN + 512;
constexpr int L_LW = L_MX + 256;
constexpr int L_MA = L_LW + 256;
constexpr int L_USED = L_MA + 2 * REC_BYTES;
constexpr int L_END = L_USED > 84 * 1024 ? L_USED : 84 * 1024;

constexpr unsigned SPIN_CAP = 1u << 18;

struct Params {
  const unsigned short* x[2];
  const unsigned char* k[2];
  const unsigned char* v[2];
  const unsigned char* wq[2];
  const unsigned char* wo[2];
  unsigned short* out;
  unsigned char* ws;
};

template <int CTRL> __device__ __forceinline__ float dpp(float x) {
  return __builtin_bit_cast(float, __builtin_amdgcn_mov_dpp(__builtin_bit_cast(int, x), CTRL, 0xf, 0xf, true));
}
constexpr int XOR1 = 0xB1, XOR2 = 0x4E, ROR4 = 0x124, ROR8 = 0x128, BCAST = 0x150;
__device__ __forceinline__ unsigned cvtpk(float lo, float hi) {
  f32x2 v = {lo, hi};
  return __builtin_bit_cast(unsigned, __builtin_convertvector(v, bf16x2));
}
__device__ __forceinline__ float bflo(unsigned u) { return __uint_as_float(u << 16); }
__device__ __forceinline__ float bfhi(unsigned u) { return __uint_as_float(u & 0xffff0000u); }
__device__ __forceinline__ float x16_sum(float x) {
  auto s = __builtin_amdgcn_permlane16_swap(__float_as_uint(x), __float_as_uint(x), false, false);
  return __uint_as_float(s[0]) + __uint_as_float(s[1]);
}
__device__ __forceinline__ float x32_sum(float x) {
  auto s = __builtin_amdgcn_permlane32_swap(__float_as_uint(x), __float_as_uint(x), false, false);
  return __uint_as_float(s[0]) + __uint_as_float(s[1]);
}
__device__ __forceinline__ float x32_tr(float a, float b) {
  auto s = __builtin_amdgcn_permlane32_swap(__float_as_uint(a), __float_as_uint(b), false, false);
  return __uint_as_float(s[0]) + __uint_as_float(s[1]);
}
__device__ __forceinline__ float x16_tr(float a, float b) {
  auto s = __builtin_amdgcn_permlane16_swap(__float_as_uint(a), __float_as_uint(b), false, false);
  return __uint_as_float(s[0]) + __uint_as_float(s[1]);
}
__device__ __forceinline__ float x16_max(float x) {
  auto s = __builtin_amdgcn_permlane16_swap(__float_as_uint(x), __float_as_uint(x), false, false);
  return fmaxf(__uint_as_float(s[0]), __uint_as_float(s[1]));
}
__device__ __forceinline__ float x32_max(float x) {
  auto s = __builtin_amdgcn_permlane32_swap(__float_as_uint(x), __float_as_uint(x), false, false);
  return fmaxf(__uint_as_float(s[0]), __uint_as_float(s[1]));
}
__device__ __forceinline__ v4u ldnt(const unsigned char* p) { return __builtin_nontemporal_load((const GAS v4u*)p); }
#define SB() __builtin_amdgcn_sched_barrier(0)
#define USE(x) do { asm volatile("" :: "v"(x)); } while (0)
#define CBAR() asm volatile("" ::: "memory")

__device__ __forceinline__ f32x4 mfma32(v4u a, v4u b, f32x4 c) {
  return __builtin_amdgcn_mfma_f32_16x16x32_bf16(__builtin_bit_cast(bf16x8, a), __builtin_bit_cast(bf16x8, b), c, 0, 0, 0);
}
__device__ __forceinline__ f32x4 mfma16(v2u a, v2u b, f32x4 c) {
  return __builtin_amdgcn_mfma_f32_16x16x16bf16_1k(__builtin_bit_cast(s16x4, a), __builtin_bit_cast(s16x4, b), c, 0, 0, 0);
}
__device__ __forceinline__ float qk_group(v4u kf, v4u qf) {
  f32x4 z = {0.f, 0.f, 0.f, 0.f};
  f32x4 d = mfma32(kf, qf, z);
  float s = dpp<0x00>(d[0]) + dpp<0x55>(d[1]);
  s += dpp<0xAA>(d[2]);
  s += dpp<0xFF>(d[3]);
  return s;
}
__device__ __forceinline__ void round_pair(float& a, float& b) {
  unsigned u = cvtpk(a, b);
  float a1 = bflo(u) * SCALE, b1 = bfhi(u) * SCALE;
  unsigned w = cvtpk(a1, b1);
  a = bflo(w); b = bfhi(w);
}

struct Tag { unsigned lo, hi8; v4u t; };
__device__ __forceinline__ unsigned g8pack(float f, unsigned hi8) { return ((__float_as_uint(f) + 0x80u) & 0xffffff00u) | hi8; }
__device__ __forceinline__ float g8val(unsigned hi) { return __uint_as_float(hi & 0xffffff00u); }
__device__ __forceinline__ unsigned g8bad(v4u g, const Tag& t) { return (g.x ^ t.lo) | (g.z ^ t.lo) | (((g.y ^ t.hi8) | (g.w ^ t.hi8)) & 0xffu); }
__device__ __forceinline__ unsigned gw(float f, unsigned t10) { return ((__float_as_uint(f) + 0x200u) & 0xfffffc00u) | t10; }
__device__ __forceinline__ v4u g16pack(float a, float b, float c, float d, const Tag& t) { return v4u{gw(a, t.t.x), gw(b, t.t.y), gw(c, t.t.z), gw(d, t.t.w)}; }
__device__ __forceinline__ float gv(unsigned w) { return __uint_as_float(w & 0xfffffc00u); }
__device__ __forceinline__ unsigned g16bad(v4u g, const Tag& t) { return ((g.x ^ t.t.x) | (g.y ^ t.t.y) | (g.z ^ t.t.z) | (g.w ^ t.t.w)) & 0x3ffu; }

__device__ __forceinline__ v4u ld_sc1(__amdgpu_buffer_rsrc_t rs, unsigned off) { return __builtin_amdgcn_raw_buffer_load_b128(rs, off, 0, 16); }
__device__ __forceinline__ void st_sc1(__amdgpu_buffer_rsrc_t rs, unsigned off, v4u v) { __builtin_amdgcn_raw_buffer_store_b128(v, rs, off, 0, 16); }

__device__ __forceinline__ void write_rec(LAS unsigned char* base, int w, int lane, f32x4 a1, f32x4 a2) {
  LAS float* rec = (LAS float*)(base) + w * 512 + (lane >> 4) * 128 + (lane & 15) * 8;
  *(LAS f32x4*)rec = a1;
  *(LAS f32x4*)(rec + 4) = a2;
}
__device__ __forceinline__ void merge_publish(const LAS unsigned char* base, int hp, int lane, __amdgpu_buffer_rsrc_t rs, int br, int hk, int slot, const Tag& tag) {
  const int h = 2 * hp + (lane >> 5), d = 4 * (lane & 31);
  const LAS float* rec = (const LAS float*)base + h * 128 + d;
  f32x4 ov[8];
#pragma unroll
  for (int w = 0; w < 8; ++w) ov[w] = *(const LAS f32x4*)(rec + w * 512);
  const f32x4 o = ((ov[0] + ov[1]) + (ov[2] + ov[3])) + ((ov[4] + ov[5]) + (ov[6] + ov[7]));
  const int hq = hk * 4 + h;
  st_sc1(rs, WS_P + (((br * 256 + hq * 8 + (d >> 4)) * 32 + slot) * 64) + (d & 15) * 4, g16pack(o[0], o[1], o[2], o[3], tag));
}

__global__ void __launch_bounds__(NTHR, 2) fused_decode(Params p) {
  __shared__ __attribute__((aligned(16))) unsigned char smem_[L_END];
  LAS unsigned char* L = (LAS unsigned char*)smem_;
  const int tid = threadIdx.x, lane = tid & 63, w = __builtin_amdgcn_readfirstlane(tid >> 6), b = blockIdx.x;
  const int h = b & 7, j = b >> 3;
  const __amdgpu_buffer_rsrc_t rs = __builtin_amdgcn_make_buffer_rsrc((void*)p.ws, 0, (int)WS_END, 0x00020000);

  const int qbr = b >> 7, qcb = (b >> 1) & 63, qhalf = b & 1;
  const int qrow0 = 256 * qhalf + 32 * w + 4 * (lane >> 3);
  const v2u xv = *(const GAS v2u*)(p.x[qbr] + qrow0);
  v4u wqr[4];
  {
    const unsigned char* wp = p.wq[qbr] + (size_t)qrow0 * 8192 + (64 * qcb + 8 * (lane & 7)) * 2;
#pragma unroll
    for (int i = 0; i < 4; ++i) wqr[i] = ldnt(wp + (size_t)i * 8192);
  }
  SB();
  __builtin_amdgcn_s_barrier();
  SB();
  const unsigned long long E = *(const GAS unsigned long long*)(p.ws + WS_EPOCH);
  Tag tag;
  {
    unsigned long long z = E + 0x9E3779B97F4A7C15ull;
    z = (z ^ (z >> 30)) * 0xBF58476D1CE4E5B9ull; z = (z ^ (z >> 27)) * 0x94D049BB133111EBull; z ^= z >> 31;
    tag.lo = (unsigned)z; tag.hi8 = (unsigned)(z >> 32) & 0xffu;
    tag.t = v4u{(unsigned)z & 0x3ffu, (unsigned)(z >> 10) & 0x3ffu, (unsigned)(z >> 20) & 0x3ffu, (unsigned)(z >> 30) & 0x3ffu};
  }
  const unsigned koff = ((lane & 15) >> 2) * 256 + (lane >> 4) * 64 + (lane & 3) * 16;
  const unsigned voff = lane * 16;
  const size_t rowA = (size_t)h * SK + 1024 * j + 16 * w;
  const unsigned char* kA = p.k[0] + rowA * 256;
  const unsigned char* vA = p.v[0] + rowA * 256;
  const size_t rowB = (size_t)h * SK + (SK - WIN) + 128 * j + 16 * w;
  const unsigned char* kB = p.k[1] + rowB * 256;
  const unsigned char* vB = p.v[1] + rowB * 256;
  v4u K[9][4], V[9][4], WO[4];
#define KADDR(u) (((u) < 16 ? kA + ((u) >> 1) * 32768 : kB) + (2 * ((u) & 1)) * 1024 + koff)
#define VADDR(u) (((u) < 16 ? vA + ((u) >> 1) * 32768 : vB) + (2 * ((u) & 1)) * 1024 + voff)
#define ISSUE_KU(u) do { K[(u) >> 1][2 * ((u) & 1)] = ldnt(KADDR(u)); K[(u) >> 1][2 * ((u) & 1) + 1] = ldnt(KADDR(u) + 1024); SB(); } while (0)
#define ISSUE_VU(u) do { V[(u) >> 1][2 * ((u) & 1)] = ldnt(VADDR(u)); V[(u) >> 1][2 * ((u) & 1) + 1] = ldnt(VADDR(u) + 1024); SB(); } while (0)
#define USE_KU(u) USE(K[(u) >> 1][2 * ((u) & 1) + 1])
#define USE_VU(u) USE(V[(u) >> 1][2 * ((u) & 1) + 1])
  ISSUE_KU(0); ISSUE_KU(1);

  {
    float acc[8];
#pragma unroll
    for (int c = 0; c < 8; ++c) acc[c] = 0.f;
    const float xs[4] = {bflo(xv.x), bfhi(xv.x), bflo(xv.y), bfhi(xv.y)};
#pragma unroll
    for (int i = 0; i < 4; ++i) {
#pragma unroll
      for (int c = 0; c < 4; ++c) { acc[2 * c] = fmaf(xs[i], bflo(wqr[i][c]), acc[2 * c]); acc[2 * c + 1] = fmaf(xs[i], bfhi(wqr[i][c]), acc[2 * c + 1]); }
    }
#pragma unroll
    for (int c = 0; c < 8; ++c) { float s = acc[c]; s += dpp<ROR8>(s); s = x16_sum(s); s = x32_sum(s); acc[c] = s; }
    if (lane < 8) {
      LAS float* r = (LAS float*)(L + L_RED) + w * 64 + lane * 8;
      *(LAS f32x4*)r = f32x4{acc[0], acc[1], acc[2], acc[3]};
      *(LAS f32x4*)(r + 4) = f32x4{acc[4], acc[5], acc[6], acc[7]};
    }
  }
  __syncthreads();
  if (w == 0 && lane < 32) {
    const LAS float* r = (const LAS float*)(L + L_RED) + 2 * lane;
    float s0 = 0.f, s1 = 0.f;
#pragma unroll
    for (int ww = 0; ww < 8; ++ww) { const f32x2 t = *(const LAS f32x2*)(r + ww * 64); s0 += t[0]; s1 += t[1]; }
    v4u g = {tag.lo, g8pack(s0, tag.hi8), tag.lo, g8pack(s1, tag.hi8)};
    st_sc1(rs, WS_QP + ((qbr * 64 + qcb) * 2 + qhalf) * 512 + lane * 16, g);
  }
  SB();
  USE_KU(0); ISSUE_KU(2);
  USE_KU(1); ISSUE_KU(3);
  USE_KU(2); ISSUE_KU(4);
  USE_KU(3); ISSUE_KU(5);
  const unsigned goff = WS_QP + (((w >> 2) * 64 + 8 * h + 2 * (w & 3) + (lane >> 5)) * 2) * 512 + (lane & 31) * 16;
  v4u g0 = ld_sc1(rs, goff), g1 = ld_sc1(rs, goff + 512);
  SB();
  USE_KU(4); ISSUE_KU(6);
  USE_KU(5); ISSUE_KU(7);
  {
    unsigned spins = 0;
    while (__any((g8bad(g0, tag) | g8bad(g1, tag)) != 0u)) {
      if (++spins > SPIN_CAP) break;
      __builtin_amdgcn_s_sleep(2);
      g0 = ld_sc1(rs, goff); g1 = ld_sc1(rs, goff + 512);
    }
    const float q0 = g8val(g0.y) + g8val(g1.y), q1 = g8val(g0.w) + g8val(g1.w);
    *(LAS unsigned*)(L + L_QS + (w >> 2) * 1024 + ((2 * (w & 3) + (lane >> 5)) * 64 + 2 * (lane & 31)) * 2) = cvtpk(q0, q1);
  }
  SB();
  USE_KU(6); ISSUE_KU(8);
  __syncthreads();
  const unsigned qldsoff = ((lane & 15) >> 2) * 256 + (4 * (lane >> 4) + (lane & 3)) * 16;
  const v4u qA = *(const LAS v4u*)(L + L_QS + qldsoff);
  const unsigned pm0 = (lane & 3) == 0 ? 0xffffu : (lane & 3) == 1 ? 0xffff0000u : 0u;
  const unsigned pm1 = (lane & 3) == 2 ? 0xffffu : (lane & 3) == 3 ? 0xffff0000u : 0u;

  float sc[9][4];
#define QK_TILE(t, q_) do { _Pragma("unroll") for (int i_ = 0; i_ < 4; ++i_) sc[t][i_] = qk_group(K[t][i_], q_); \
                        round_pair(sc[t][0], sc[t][1]); round_pair(sc[t][2], sc[t][3]); SB(); } while (0)
  QK_TILE(0, qA); USE_KU(7); ISSUE_KU(9);
  QK_TILE(1, qA); USE_KU(8); ISSUE_KU(10);
  QK_TILE(2, qA); USE_KU(9); ISSUE_KU(11);
  QK_TILE(3, qA); USE_KU(10); ISSUE_KU(12);
  USE_KU(11); ISSUE_KU(13);
  QK_TILE(4, qA); USE_KU(12); ISSUE_KU(14);
  QK_TILE(5, qA); USE_KU(13); ISSUE_KU(15);
  USE_KU(14); ISSUE_KU(16);
  QK_TILE(6, qA); USE_KU(15); ISSUE_KU(17);
  QK_TILE(7, qA); USE_KU(16); ISSUE_VU(0);
  const v4u qB = *(const LAS v4u*)(L + L_QS + 1024 + qldsoff);
  USE_KU(17); ISSUE_VU(1);
  QK_TILE(8, qB);
  {
    float mA = -INFINITY;
#pragma unroll
    for (int t = 0; t < 8; ++t) mA = fmaxf(mA, fmaxf(fmaxf(sc[t][0], sc[t][1]), fmaxf(sc[t][2], sc[t][3])));
    float mB = fmaxf(fmaxf(sc[8][0], sc[8][1]), fmaxf(sc[8][2], sc[8][3]));
    mA = x16_max(mA); mA = x32_max(mA); mB = x16_max(mB); mB = x32_max(mB);
    if (lane < 16 && (lane & 3) == 0) { LAS float* mx = (LAS float*)(L + L_MX) + (lane >> 2) * 8 + w; mx[0] = mA; mx[32] = mB; }
  }
  __syncthreads();
  float MA, MB;
  {
    const LAS float* mx = (const LAS float*)(L + L_MX) + ((lane & 15) >> 2) * 8;
    const f32x4 x0 = *(const LAS f32x4*)mx, x1 = *(const LAS f32x4*)(mx + 4), y0 = *(const LAS f32x4*)(mx + 32), y1 = *(const LAS f32x4*)(mx + 36);
    MA = fmaxf(fmaxf(fmaxf(x0[0], x0[1]), fmaxf(x0[2], x0[3])), fmaxf(fmaxf(x1[0], x1[1]), fmaxf(x1[2], x1[3])));
    MB = fmaxf(fmaxf(fmaxf(y0[0], y0[1]), fmaxf(y0[2], y0[3])), fmaxf(fmaxf(y1[0], y1[1]), fmaxf(y1[2], y1[3])));
  }
  unsigned pp[9][4];
  {
    float lA = 0.f, lB = 0.f;
    const float mbA = -MA * LOG2E, mbB = -MB * LOG2E;
#pragma unroll
    for (int t = 0; t < 9; ++t) {
#pragma unroll
      for (int i = 0; i < 4; ++i) { const float pe = __builtin_amdgcn_exp2f(fmaf(sc[t][i], LOG2E, t < 8 ? mbA : mbB)); if (t < 8) lA += pe; else lB += pe; pp[t][i] = cvtpk(pe, pe); }
      SB();
      if (t == 3) { USE_VU(0); ISSUE_VU(2); }
    }
    lA = x16_sum(lA); lA = x32_sum(lA); lB = x16_sum(lB); lB = x32_sum(lB);
    if (lane < 16 && (lane & 3) == 0) { LAS float* lw = (LAS float*)(L + L_LW) + (lane >> 2) * 8 + w; lw[0] = lA; lw[32] = lB; }
  }
  SB();
  const unsigned wooff = (lane >> 4) * 1024 + (lane & 15) * 16;
  const unsigned char* wob = p.wo[w >> 2] + (size_t)(16 * b) * 1024 + 128 * (w & 3) * 2;
#define ISSUE_WO(half) do { WO[2 * (half)] = ldnt(wob + (half) * 8192 + wooff); WO[2 * (half) + 1] = ldnt(wob + (half) * 8192 + 4096 + wooff); SB(); } while (0)
  const int isB = w >> 2, cg = w & 3, gq = lane >> 4;
  const unsigned mo = WS_M + ((((w & 1) * 32 + (b >> 3)) * 2 + (b & 1)) * 32 + (lane & 31)) * 16;
  v4u gm = {0u, 0u, 0u, 0u};
  f32x4 a1 = {0.f, 0.f, 0.f, 0.f}, a2 = {0.f, 0.f, 0.f, 0.f}, b1 = {0.f, 0.f, 0.f, 0.f}, b2 = {0.f, 0.f, 0.f, 0.f};
#pragma unroll
  for (int u = 0; u < 18; ++u) {
    const int t = u >> 1;
#pragma unroll
    for (int i = 2 * (u & 1); i < 2 * (u & 1) + 2; ++i) {
      const v2u pa = {pp[t][i] & pm0, pp[t][i] & pm1};
      if (u < 16) { a1 = mfma16(pa, v2u{V[t][i].x, V[t][i].y}, a1); a2 = mfma16(pa, v2u{V[t][i].z, V[t][i].w}, a2); }
      else { b1 = mfma16(pa, v2u{V[t][i].x, V[t][i].y}, b1); b2 = mfma16(pa, v2u{V[t][i].z, V[t][i].w}, b2); }
    }
    SB();
    if (u == 2) {
      __syncthreads();
      float Lt = 0.f;
      const LAS float* lw = (const LAS float*)(L + L_LW) + (lane & 7) * 8;
      if (w == 7) { const f32x4 x0 = *(const LAS f32x4*)lw, x1 = *(const LAS f32x4*)(lw + 4); Lt = ((x0[0] + x0[1]) + (x0[2] + x0[3])) + ((x1[0] + x1[1]) + (x1[2] + x1[3])); }
      const float Ma_ = __shfl(MA, 4 * (lane & 3)), Mb_ = __shfl(MB, 4 * (lane & 3));
      const float Mt = (lane & 4) ? Mb_ : Ma_;
      st_sc1(rs, (w == 7 && lane < 16) ? WS_M + (((((lane >> 2) & 1) * 32 + h * 4 + (lane & 3)) * 2 + (lane >> 3)) * 32 + j) * 16 : 0x7ffffff0u, g16pack(Mt, Lt, Mt, Lt, tag));
      SB();
    }
    if (u == 11) { gm = ld_sc1(rs, mo); SB(); }
    if (u == 15) write_rec(L + L_MA, w, lane, a1, a2);
    if (u + 3 < 18) { ISSUE_VU(u + 3); }
  }
  write_rec(L + L_MA + REC_BYTES, w, lane, b1, b2);
  __syncthreads();
  if (w < 4) merge_publish(L + L_MA + (w >> 1) * REC_BYTES, w & 1, lane, rs, w >> 1, h, j, tag);
  SB();
  ISSUE_WO(0); ISSUE_WO(1);
  if (w == 4 || w == 5) {
    const int cb = w - 4;
    const unsigned pb = WS_P + (cb * 256 + b) * 2048 + lane * 16;
    unsigned spins = 0;
    while (__any(g16bad(gm, tag) != 0u)) {
      if (++spins > SPIN_CAP) break;
      CBAR();
      gm = ld_sc1(rs, mo);
    }
    const float m = gv(gm.x);
    float M = fmaxf(m, dpp<XOR1>(m)); M = fmaxf(M, dpp<XOR2>(M)); M = fmaxf(M, dpp<ROR4>(M)); M = fmaxf(M, dpp<ROR8>(M)); M = x16_max(M);
    float e = __builtin_amdgcn_exp2f((m - M) * LOG2E);
    float Ls = e * gv(gm.y);
    Ls += dpp<XOR1>(Ls); Ls += dpp<XOR2>(Ls); Ls += dpp<ROR4>(Ls); Ls += dpp<ROR8>(Ls); Ls = x16_sum(Ls);
    e *= __builtin_amdgcn_rcpf(Ls);
    const float e0 = __shfl(e, lane >> 2), e1 = __shfl(e, 16 + (lane >> 2));
    v4u g0_, g1_;
    while (true) {
      CBAR();
      g0_ = ld_sc1(rs, pb); g1_ = ld_sc1(rs, pb + 1024);
      if (!__any((g16bad(g0_, tag) | g16bad(g1_, tag)) != 0u)) break;
      if (++spins > SPIN_CAP) break;
    }
    const float o0 = fmaf(e0, gv(g0_.x), e1 * gv(g1_.x)), o1 = fmaf(e0, gv(g0_.y), e1 * gv(g1_.y)), o2 = fmaf(e0, gv(g0_.z), e1 * gv(g1_.z)), o3 = fmaf(e0, gv(g0_.w), e1 * gv(g1_.w));
    const float t0 = x32_tr(o0, o1), t1 = x32_tr(o2, o3);
    float u_ = x16_tr(t0, t1);
    u_ += dpp<ROR4>(u_); u_ += dpp<ROR8>(u_);
    const unsigned ob = cvtpk(u_, u_);
    if ((lane & 12) == 0) ((LAS unsigned short*)(L + L_OS))[cb * 16 + 4 * (lane & 3) + 2 * ((lane >> 4) & 1) + (lane >> 5)] = (unsigned short)ob;
  }
  __syncthreads();
  {
    f32x4 c1 = {0.f, 0.f, 0.f, 0.f}, c2 = {0.f, 0.f, 0.f, 0.f};
#pragma unroll
    for (int i = 0; i < 4; ++i) {
      const unsigned t = ((const LAS unsigned*)(L + L_OS))[isB * 8 + 2 * i + (gq >> 1)];
      const unsigned oq = (gq & 1) ? ((t >> 16) | (t & 0xffff0000u)) : ((t << 16) | (t & 0xffffu));
      const v2u pa = {oq & pm0, oq & pm1};
      c1 = mfma16(pa, v2u{WO[i].x, WO[i].y}, c1); c2 = mfma16(pa, v2u{WO[i].z, WO[i].w}, c2);
    }
    const f32x4 c = (gq & 1) ? c2 : c1;
    const int col = 128 * cg + 8 * (lane & 15) + 4 * (gq & 1);
    st_sc1(rs, gq < 2 ? WS_OP + ((isB * 32 + (col >> 4)) * 256 + b) * 64 + (col & 15) * 4 : 0x7ffffff0u, g16pack(c[0], c[1], c[2], c[3], tag));
  }
  if (b < 64) {
    const unsigned fo = WS_OP + b * 16384 + tid * 16;
    v4u g0_, g1_;
    unsigned spins = 0;
    while (true) {
      CBAR();
      g0_ = ld_sc1(rs, fo); g1_ = ld_sc1(rs, fo + 8192);
      if (!__any((g16bad(g0_, tag) | g16bad(g1_, tag)) != 0u)) break;
      if (++spins > SPIN_CAP) break;
    }
    float s[4] = {gv(g0_.x) + gv(g1_.x), gv(g0_.y) + gv(g1_.y), gv(g0_.z) + gv(g1_.z), gv(g0_.w) + gv(g1_.w)};
#pragma unroll
    for (int k = 0; k < 4; ++k) { s[k] += dpp<ROR4>(s[k]); s[k] += dpp<ROR8>(s[k]); s[k] = x16_sum(s[k]); s[k] = x32_sum(s[k]); }
    if (lane < 4) *(LAS f32x4*)((LAS float*)(L + L_FIN) + w * 16 + 4 * lane) = f32x4{s[0], s[1], s[2], s[3]};
    __syncthreads();
    if (tid < 8) {
      float t0 = 0.f, t1 = 0.f;
#pragma unroll
      for (int ww = 0; ww < 8; ++ww) { const f32x2 t = *(const LAS f32x2*)((const LAS float*)(L + L_FIN) + ww * 16 + 2 * tid); t0 += t[0]; t1 += t[1]; }
      *(GAS unsigned*)(p.out + (b >> 5) * 512 + (b & 31) * 16 + 2 * tid) = cvtpk(t0, t1);
    }
    if (b == 0 && tid == 0) *(GAS unsigned long long*)(p.ws + WS_EPOCH) = E + 1;
  }
}

extern "C" void kernel_launch(void* const* d_in, const int* in_sizes, int n_in,
                              void* d_out, int out_size, void* d_ws, size_t ws_size,
                              hipStream_t stream) {
  Params p;
  p.x[0] = (const unsigned short*)d_in[0]; p.k[0] = (const unsigned char*)d_in[1]; p.v[0] = (const unsigned char*)d_in[2];
  p.wq[0] = (const unsigned char*)d_in[3]; p.wo[0] = (const unsigned char*)d_in[4];
  p.x[1] = (const unsigned short*)d_in[5]; p.k[1] = (const unsigned char*)d_in[6]; p.v[1] = (const unsigned char*)d_in[7];
  p.wq[1] = (const unsigned char*)d_in[8]; p.wo[1] = (const unsigned char*)d_in[9];
  p.out = (unsigned short*)d_out; p.ws = (unsigned char*)d_ws;
  fused_decode<<<NWG, NTHR, 0, stream>>>(p);
}
```
